```python
import torch

def _softmax(x, axis=-1):
    return torch.softmax(x, dim=axis)

def _gelu(x):
    return torch.nn.functional.gelu(x, approximate='tanh')

def _silu(x):
    return torch.nn.functional.silu(x)

def _sigmoid(x):
    return torch.sigmoid(x)

def _layernorm(x, axis=-1, eps=1e-05):
    m = x.mean(dim=axis, keepdim=True)
    v = x.var(dim=axis, keepdim=True, unbiased=False)
    return (x - m) / torch.sqrt(v + eps)

def _scatter_add(tgt, idx, src, axis=0):
    return tgt.clone().index_add_(axis, idx, src)

def _segment_sum(src, seg, n):
    out = torch.zeros((n,) + src.shape[1:], dtype=src.dtype)
    return out.index_add_(0, seg, src)

def _pad(x, pw):
    flat = tuple((v for pair in reversed(pw) for v in pair))
    return torch.nn.functional.pad(x, flat)
kernel_name = 'adaLN_cond__spectral_fnet__dense_activation_relu__prompt'

def setup_inputs():

    _w = lambda a: (lambda t: t.to(torch.bfloat16) if t.is_floating_point() else t)(torch.as_tensor(a))

    def _A_setup():
        B, S, D = (1, 2048, 512)
        ns = torch.arange(S)[:, None] * torch.arange(S)[None, :]
        nd = torch.arange(D)[:, None] * torch.arange(D)[None, :]
        Ws_re = torch.cos(-2.0 * torch.pi * ns / S).to(float)
        Ws_im = torch.sin(-2.0 * torch.pi * ns / S).to(float)
        Wd_re = torch.cos(-2.0 * torch.pi * nd / D).to(float)
        Wd_im = torch.sin(-2.0 * torch.pi * nd / D).to(float)
        return {'x': _w((lambda *s: torch.randn(*s, dtype=torch.bfloat16) * 0.1)(B, S, D)), 'dft_seq_re': _w(Ws_re), 'dft_seq_im': _w(Ws_im), 'dft_hid_re': _w(Wd_re), 'dft_hid_im': _w(Wd_im)}
    _A_in = _A_setup()

    def _B_setup():
        B, S, D, Dff = (1, 2048, 512, 1376)
        return {'x': _w((lambda *s: torch.randn(*s, dtype=torch.bfloat16) * 0.1)(B, S, D)), 'w1': _w((lambda *s: torch.randn(*s, dtype=torch.bfloat16) * 3.2)(D, Dff)), 'w2': _w((lambda *s: torch.randn(*s, dtype=torch.bfloat16) * 3.2)(Dff, D))}
    _B_in = _B_setup()
    return {'A_x': _A_in['x'], 'A_dft_seq_re': _A_in['dft_seq_re'], 'A_dft_seq_im': _A_in['dft_seq_im'], 'A_dft_hid_re': _A_in['dft_hid_re'], 'A_dft_hid_im': _A_in['dft_hid_im'], 'B_x': _B_in['x'], 'B_w1': _B_in['w1'], 'B_w2': _B_in['w2'], 'c': _w((lambda *s: torch.randn(*s, dtype=torch.bfloat16) * 0.2)(1, 512) * 8.0), 'w_mod': _w((lambda *s: torch.randn(*s, dtype=torch.bfloat16) * 0.2)(512, 6 * 512) * 512 ** (-0.5) * 8.0)}

def reference(A_x, A_dft_seq_re, A_dft_seq_im, A_dft_hid_re, A_dft_hid_im, B_x, B_w1, B_w2, c, w_mod):

    def _A_ref(x, dft_seq_re, dft_seq_im, dft_hid_re, dft_hid_im):
        a = torch.einsum('bsd,ed->bse', x, dft_hid_re)
        b = torch.einsum('bsd,ed->bse', x, dft_hid_im)
        y = torch.einsum('ts,bse->bte', dft_seq_re, a) - torch.einsum('ts,bse->bte', dft_seq_im, b)
        return y

    def _B_ref(x, w1, w2):
        h = torch.einsum('bsd,df->bsf', x, w1)
        zero = torch.zeros_like(h)
        h = torch.maximum(h, zero)
        return torch.einsum('bsf,fd->bsd', h, w2)
    x = A_x
    mod = (_silu(c) @ w_mod).reshape(c.shape[0], 6, 1, -1)
    sa, ca, ga, sb, cb, gb = (mod[:, 0].clone(), mod[:, 1].clone(), mod[:, 2].clone(), mod[:, 3].clone(), mod[:, 4].clone(), mod[:, 5].clone())
    h = _layernorm(x, axis=-1) * (1 + ca) + sa
    x = x + ga * _A_ref(h, A_dft_seq_re, A_dft_seq_im, A_dft_hid_re, A_dft_hid_im)
    h = _layernorm(x, axis=-1) * (1 + cb) + sb
    x = x + gb * _B_ref(h, B_w1, B_w2)
    return x
```

```cpp
#include <hip/hip_runtime.h>
#include <hip/hip_bf16.h>

typedef unsigned u32;
typedef unsigned long long u64;
typedef u32 v4u __attribute__((ext_vector_type(4)));
typedef u32 v2u __attribute__((ext_vector_type(2)));
typedef int v4i __attribute__((ext_vector_type(4)));
typedef float v4f __attribute__((ext_vector_type(4)));
typedef float v2f __attribute__((ext_vector_type(2)));
typedef __bf16 bf2 __attribute__((ext_vector_type(2)));
#define LAS __attribute__((address_space(3)))
#define DEV __device__ __forceinline__

constexpr int S = 2048, D = 512, F = 1376;
constexpr u32 WS_E = 0;
constexpr u32 WS_FLAGW = 8192;
constexpr u32 WS_FLAG0R = 24576;
constexpr u32 WS_MODG = 32768;
constexpr u32 WS_STAT = 65536;
constexpr u32 WS_MODP = 1024 << 10;
constexpr u32 WS_RS = 640 << 10;
constexpr u32 WS_XFL = 1536 << 10;
constexpr u32 WS_FLG = 768 << 10;
constexpr u32 XN_KS = 32768 + 0;
constexpr u32 WD_NT = 32768 + 0;
constexpr u32 W1_NT = 16384 + 0;
constexpr u32 W2_NT = 43 * 1024 + 0;
constexpr u32 PQ_KS = 4096, PQ_TB = 32 * PQ_KS;
constexpr u32 WS_XCH = 16u << 20;
constexpr u32 X1_TB = 65536 + 0;
constexpr u32 U_TB = 43 * 4096 + 0;
constexpr u32 ST_TB = 4096 + 0, RS_TB = 2048 + 0;
constexpr u32 WS_XNF = 2u << 20, WS_WDF = 5u << 20, WS_W1F = 7u << 20, WS_W2F = 9u << 20, WS_PQF = 11u << 20;
constexpr u32 WS_X1F = 21u << 20, WS_UF = 24u << 20, WS_TOTAL = 31u << 20;
static_assert(64 * XN_KS <= (3u << 20) && 32 * WD_NT <= (2u << 20) && 88 * W1_NT <= (2u << 20) && 32 * W2_NT <= (2u << 20), "");
static_assert(32 * PQ_TB <= (10u << 20) && 32 * X1_TB <= (3u << 20) && 32 * U_TB <= (7u << 20) && 32 * ST_TB <= (256u << 10) && 32 * RS_TB <= (128u << 10), "");
constexpr u32 OOB = 0x40000000u;
constexpr u32 L_RING = 0;
constexpr u32 L_AUX = 131072;
constexpr u32 L_LN = 57344;
constexpr u32 L_MOD = L_AUX;
constexpr u32 L_RSX = L_AUX + 15360;
constexpr u32 L_PX = L_AUX + 16384;
constexpr u32 L_A2 = 65536;
constexpr u32 LDS_BYTES = 163840;
constexpr u32 CODE_BYTES = 30720;
constexpr u32 SPIN_CAP = 400000;

DEV float asf(u32 x) { return __builtin_bit_cast(float, x); }
DEV u32 asu(float x) { return __builtin_bit_cast(u32, x); }
DEV u32 rfl(u32 x) { return __builtin_amdgcn_readfirstlane(x); }
DEV v4i make_srd(const void* p, u32 bytes) {
  u64 a = (u64)p;
  v4i r;
  r.x = (int)(u32)a; r.y = (int)(u32)(a >> 32); r.z = (int)bytes; r.w = 0x00020000;
  return r;
}
DEV u32 pk2(float lo, float hi) {
  v2f v = {lo, hi};
  bf2 h = __builtin_convertvector(v, bf2);
  return __builtin_bit_cast(u32, h);
}
DEV float bflo(u32 v) { return asf(v << 16); }
DEV float bfhi(u32 v) { return asf(v & 0xffff0000u); }
DEV float rbf(float v) { return bflo(pk2(v, 0.f)); }

#define DMA(m0v, voff, srd, soff) \
  asm volatile("s_mov_b32 m0, %0\n\ts_nop 0\n\tbuffer_load_dwordx4 %1, %2, %3 offen lds" ::"s"(m0v), "v"(voff), "s"(srd), "s"(soff) : "memory")
#define DMAI(m0base, imm, voff, srd, soff) \
  asm volatile("s_add_u32 m0, %0, %1\n\ts_nop 0\n\tbuffer_load_dwordx4 %2, %3, %4 offen lds" ::"s"(m0base), "i"(imm), "v"(voff), "s"(srd), "s"(soff) : "memory", "scc")
#define LD4V(dst, voff, srd, soff) \
  asm volatile("buffer_load_dwordx4 %0, %1, %2, %3 offen" : "=v"(dst) : "v"(voff), "s"(srd), "s"(soff) : "memory")
#define LD2V(dst, voff, srd, soff) \
  asm volatile("buffer_load_dwordx2 %0, %1, %2, %3 offen" : "=v"(dst) : "v"(voff), "s"(srd), "s"(soff) : "memory")
#define LD4V_SC1(dst, voff, srd, soff) \
  asm volatile("buffer_load_dwordx4 %0, %1, %2, %3 offen sc1" : "=v"(dst) : "v"(voff), "s"(srd), "s"(soff) : "memory")
#define LD1V_SC1(dst, voff, srd, soff) \
  asm volatile("buffer_load_dword %0, %1, %2, %3 offen sc1" : "=v"(dst) : "v"(voff), "s"(srd), "s"(soff) : "memory")
#define ST4_SC1(val, voff, srd, soff) \
  asm volatile("buffer_store_dwordx4 %0, %1, %2, %3 offen sc1\n\ts_nop 1" ::"v"(val), "v"(voff), "s"(srd), "s"(soff) : "memory")
#define ST4(val, voff, srd, soff) \
  asm volatile("buffer_store_dwordx4 %0, %1, %2, %3 offen\n\ts_nop 1" ::"v"(val), "v"(voff), "s"(srd), "s"(soff) : "memory")
#define ST2_SC1(val, voff, srd, soff) \
  asm volatile("buffer_store_dwordx2 %0, %1, %2, %3 offen sc1" ::"v"(val), "v"(voff), "s"(srd), "s"(soff) : "memory")
#define ST1_SC1(val, voff, srd, soff) \
  asm volatile("buffer_store_dword %0, %1, %2, %3 offen sc1" ::"v"(val), "v"(voff), "s"(srd), "s"(soff) : "memory")
#define WAITV(n) asm volatile("s_waitcnt vmcnt(%0)" ::"i"(n) : "memory")
#define WAITL(n) asm volatile("s_waitcnt lgkmcnt(%0)" ::"i"(n) : "memory")
#define PINV(x) asm volatile("" : "+v"(x))
#define PINA(x) asm volatile("" : "+a"(x))
#define SB() __builtin_amdgcn_sched_barrier(0)
#define BAR() do { asm volatile("s_waitcnt lgkmcnt(0)\n\ts_barrier" ::: "memory"); } while (0)

DEV v4u lds_rd4(u32 a) { return *(const LAS v4u*)(a); }
DEV v4f lds_rd4f(u32 a) { return *(const LAS v4f*)(a); }
DEV float lds_rd1f(u32 a) { return *(const LAS float*)(a); }
DEV v2u lds_rd2(u32 a) { return *(const LAS v2u*)(a); }
DEV void lds_wr4(u32 a, v4u v) { *(LAS v4u*)(a) = v; }
DEV void lds_wr4f(u32 a, v4f v) { *(LAS v4f*)(a) = v; }
DEV void lds_wr2(u32 a, v2u v) { *(LAS v2u*)(a) = v; }
DEV void lds_wr1f(u32 a, float v) { *(LAS float*)(a) = v; }

template <int CTRL> DEV float dpp_add(float x) {
  return x + asf((u32)__builtin_amdgcn_update_dpp(0, (int)asu(x), CTRL, 0xf, 0xf, true));
}
template <int CTRL> DEV float dpp_mov(float x) {
  return asf((u32)__builtin_amdgcn_update_dpp(0, (int)asu(x), CTRL, 0xf, 0xf, true));
}
DEV float sum_rows(float x) {
  u32 u = asu(x);
  auto a = __builtin_amdgcn_permlane16_swap(u, u, false, false);
  float y = asf((u32)a[0]) + asf((u32)a[1]);
  u = asu(y);
  auto c = __builtin_amdgcn_permlane32_swap(u, u, false, false);
  return asf((u32)c[0]) + asf((u32)c[1]);
}
DEV float wave_sum(float x) {
  x = dpp_add<0xB1>(x);
  x = dpp_add<0x4E>(x);
  x = dpp_add<0x141>(x);
  x = dpp_add<0x140>(x);
  return sum_rows(x);
}
DEV v4u swap_pack(v2u X, v2u Y) {
  auto a = __builtin_amdgcn_permlane16_swap(X.x, Y.x, false, false);
  auto c = __builtin_amdgcn_permlane16_swap(X.y, Y.y, false, false);
  v4u r;
  r.x = a[0]; r.y = c[0]; r.z = a[1]; r.w = c[1];
  return r;
}

DEV void ln_row(v4u raw, u32 lds_addr) {
  float v[8];
  v[0] = bflo(raw.x); v[1] = bfhi(raw.x); v[2] = bflo(raw.y); v[3] = bfhi(raw.y);
  v[4] = bflo(raw.z); v[5] = bfhi(raw.z); v[6] = bflo(raw.w); v[7] = bfhi(raw.w);
  float s = ((v[0] + v[1]) + (v[2] + v[3])) + ((v[4] + v[5]) + (v[6] + v[7]));
  float mean = wave_sum(s) * (1.f / D);
  float q = 0.f;
#pragma unroll
  for (int i = 0; i < 8; i++) { v[i] -= mean; q += v[i] * v[i]; }
  float rs = __builtin_amdgcn_rsqf(wave_sum(q) * (1.f / D) + 1e-5f);
  v4u o;
  o.x = pk2(v[0] * rs, v[1] * rs); o.y = pk2(v[2] * rs, v[3] * rs);
  o.z = pk2(v[4] * rs, v[5] * rs); o.w = pk2(v[6] * rs, v[7] * rs);
  lds_wr4(lds_addr, o);
}

#define LD4V_SC1I(dst, voff, srd, soff, imm) \
  asm volatile("buffer_load_dwordx4 %0, %1, %2, %3 offen offset:%4 sc1" : "=v"(dst) : "v"(voff), "s"(srd), "s"(soff), "i"(imm) : "memory")
#define LD4A_SC1I(dst, voff, srd, soff, imm) \
  asm volatile("buffer_load_dwordx4 %0, %1, %2, %3 offen offset:%4 sc1" : "=a"(dst) : "v"(voff), "s"(srd), "s"(soff), "i"(imm) : "memory")

#define DMAI_SC1(m0base, imm, voff, srd, soff) \
  asm volatile("s_add_u32 m0, %0, %1\n\ts_nop 0\n\tbuffer_load_dwordx4 %2, %3, %4 offen sc1 lds" ::"s"(m0base), "i"(imm), "v"(voff), "s"(srd), "s"(soff) : "memory", "scc")
#define BARN() asm volatile("s_barrier" ::: "memory")
#define SPIN(cond, body)                                                       \
  if (__builtin_expect((cond), 0)) {                                           \
    _Pragma("clang loop unroll(disable)") for (u32 it = 0; it < SPIN_CAP; it++) { body; if (!(cond)) break; } \
  }
#define MF(acc, ca, a, cb, b) asm volatile("v_mfma_f32_16x16x32_bf16 %0, %1, %2, %0" : "+a"(acc) : ca(a), cb(b))
#define MF0(acc, ca, a, cb, b) asm volatile("v_mfma_f32_16x16x32_bf16 %0, %1, %2, 0" : "=&a"(acc) : ca(a), cb(b))
#define EQ4(f) ((f).x == tag && (f).y == tag && (f).z == tag && (f).w == tag)
#define POLL4(f, voff, soff) \
  asm volatile("buffer_load_dwordx4 %0, %1, %2, %3 offen sc1\n\ts_waitcnt vmcnt(0)" : "=&v"(f) : "v"(voff), "s"(sws), "s"(soff) : "memory")
#define POLL1(f, voff, soff) \
  asm volatile("buffer_load_dword %0, %1, %2, %3 offen sc1\n\ts_waitcnt vmcnt(0)" : "=&v"(f) : "v"(voff), "s"(sws), "s"(soff) : "memory")

DEV v4u rs_piece(float p, float q) {
  const u32 hh = pk2(p, q);
  const u32 ll = pk2(p - bflo(hh), q - bfhi(hh));
  v4u o;
  o.x = (hh & 0xffffu) | (ll << 16); o.y = hh; o.z = (ll >> 16) | (hh & 0xffff0000u); o.w = 0;
  return o;
}
DEV v4u z_piece(float zr, float zi, u32 g4) {
  const u32 hh = pk2(zr, -zi);
  const u32 ll = pk2(zr - bflo(hh), -zi - bfhi(hh));
  v4u o;
  o.x = (hh & 0xffffu) | (hh << 16); o.y = (ll & 0xffffu) | (hh & 0xffff0000u); o.z = (hh >> 16) | (ll & 0xffff0000u); o.w = 0;
  if (g4 >= 2) { o.x = 0; o.y = 0; o.z = 0; }
  return o;
}
DEV u32 relu_pk(u32 v) {
  typedef short s2 __attribute__((ext_vector_type(2)));
  const s2 z = {0, 0};
  const s2 m = __builtin_elementwise_max(__builtin_bit_cast(s2, v), z);
  return __builtin_bit_cast(u32, m);
}

__global__ __launch_bounds__(256, 1) void fused_kernel(const void* __restrict__ px, const void* __restrict__ pwre,
                                                       const void* __restrict__ pwim, const void* __restrict__ pdre,
                                                       const void* __restrict__ pdim, const void* __restrict__ pw1,
                                                       const void* __restrict__ pw2, const void* __restrict__ pc,
                                                       const void* __restrict__ pwm, void* __restrict__ pout,
                                                       void* __restrict__ pws) {
  extern __shared__ __attribute__((aligned(16))) char smem[];
  u64 pc0;
  asm volatile("s_getpc_b64 %0" : "=s"(pc0));
  const u32 tid = threadIdx.x, lane = tid & 63;
  const u32 w = rfl(tid >> 6);
  const u32 b = blockIdx.x;
  const u32 l16 = lane * 16, zero = 0;
  const u32 rg = b;
  const u32 xcd = b & 7, idx = b >> 3;
  const v4i scode = make_srd((const void*)(pc0 & ~255ull), CODE_BYTES);
  v4u cpf;
  LD4V(cpf, l16, scode, w == 3 ? idx * 1024 : OOB);
  SB();
  const v4i sx = make_srd(px, S * D * 2);
  const v4i sws = make_srd(pws, WS_TOTAL);
  const v4i sout = make_srd(pout, S * D * 2);
  u32 tagv; v4u xr0, xr1;
  LD4V(xr0, l16, sx, (8 * rg + 2 * w) * 1024);
  LD4V(xr1, l16, sx, (8 * rg + 2 * w + 1) * 1024);
  LD1V_SC1(tagv, zero, sws, WS_E);
  SB();
  const u32 g = idx >> 2, j = (idx & 3) * 2 + (xcd >> 2), tb = (xcd & 3) * 8 + g;
  const u32 g4 = lane >> 4, r = lane & 15;
  const u32 lds0 = (u32)(size_t)(LAS char*)smem;
  const u32 cj = j >> 1, kh = j & 1, nh = w & 1, kq = w >> 1;
  const v4i sre = make_srd(kh ? pwre : pwim, S * S * 2), sim = make_srd(kh ? pwim : pwre, S * S * 2);
  const u32 ph = (cj + g) & 1;
  const u32 voffA = (lane >> 3) * 4096 + (((lane & 7) ^ (lane >> 3)) * 16);
  const u32 lane0 = lane == 0 ? 0u : OOB;
  const u32 ringq = lds0 + L_RING + kq * 65536, ringw = ringq + nh * 4096;
#define KB(i) (16 * kh + kq + 2 * ((i) ^ ph))
  u32 sR[4];
#pragma unroll
  for (int q = 0; q < 4; q++) sR[q] = (tb * 64 + 8 * ((4 * nh + q + 4 * kq) & 7)) * 4096;
  u32 vA0 = voffA + KB(0) * 128; PINV(vA0);
  DMAI(ringw, 0, vA0, sre, sR[0]);
  DMAI(ringw, 1024, vA0, sre, sR[1]);
  DMAI(ringw, 2048, vA0, sre, sR[2]);
  DMAI(ringw, 3072, vA0, sre, sR[3]);
  DMAI(ringw, 8192, vA0, sim, sR[0]);
  DMAI(ringw, 9216, vA0, sim, sR[1]);
  DMAI(ringw, 10240, vA0, sim, sR[2]);
  DMAI(ringw, 11264, vA0, sim, sR[3]);
  WAITV(8);
  PINV(tagv); PINV(xr0); PINV(xr1); PINV(cpf);
  const u32 tag = rfl(tagv) + 1u;
  ln_row(xr0, lds0 + L_LN + 2 * w * 1024 + (l16 ^ (((2 * w) & 3) << 6)));
  ln_row(xr1, lds0 + L_LN + (2 * w + 1) * 1024 + (l16 ^ (((2 * w + 1) & 3) << 6)));

  BAR();
  {
    const u32 qq = r >> 2, p = r & 3;
    const u32 a0 = lds0 + L_LN + qq * 1024 + w * 256 + ((g4 * 32 + p * 8) ^ (qq << 6)), a1 = a0 ^ 128u;
    v2u t0, t1, t2, t3;
    asm volatile("ds_read_b64_tr_b16 %0, %4\n\tds_read_b64_tr_b16 %1, %4 offset:4096\n\tds_read_b64_tr_b16 %2, %5\n\t"
                 "ds_read_b64_tr_b16 %3, %5 offset:4096\n\ts_waitcnt lgkmcnt(0)"
                 : "=&v"(t0), "=&v"(t1), "=&v"(t2), "=&v"(t3) : "v"(a0), "v"(a1) : "memory");
    const u32 vo = g4 * 1024 + r * 16;
    const u32 so = WS_XNF + (rg >> 2) * XN_KS + (rg & 3) * 256 + w * 8192;
    v4u o; o.x = t0.x; o.y = t0.y; o.z = t1.x; o.w = t1.y;
    ST4_SC1(o, vo, sws, so);
    v4u o2; o2.x = t2.x; o2.y = t2.y; o2.z = t3.x; o2.w = t3.y;
    ST4_SC1(o2, vo, sws, so + 4096);
  }
  u32 vA1 = voffA + KB(1) * 128; PINV(vA1);
  DMAI(ringw, 16384, vA1, sre, sR[0]);
  DMAI(ringw, 17408, vA1, sre, sR[1]);
  DMAI(ringw, 18432, vA1, sre, sR[2]);
  DMAI(ringw, 19456, vA1, sre, sR[3]);
  DMAI(ringw, 24576, vA1, sim, sR[0]);
  DMAI(ringw, 25600, vA1, sim, sR[1]);
  DMAI(ringw, 26624, vA1, sim, sR[2]);
  DMAI(ringw, 27648, vA1, sim, sR[3]);
  SB();
  const u32 slot = idx * 2 + (w & 1);
  const u32 scr = lds0 + L_AUX + 8192 * w;
  const void* usrc; u32 ubytes, vsrc, sbase, sstep, dbase, dstep, nfr, vdst = l16;
  {
    const u32 u1 = 22 * xcd + slot, ks1 = u1 / 11, cq1 = u1 - ks1 * 11;
    const u32 u2 = 22 * xcd + (slot - 22), ks2 = u2 >> 2, cq2 = u2 & 3;
    const u32 u3 = 16 * xcd + (slot - 44), im = u3 >> 6, nt = (u3 >> 1) & 31, hf = u3 & 1;
    const bool t1 = slot < 22, t2 = slot < 44;
    usrc = t1 ? pw1 : t2 ? pw2 : im ? pdim : pdre;
    ubytes = t2 ? D * F * 2 : D * D * 2;
    const u32 rowb = t1 ? F * 2 : D * 2;
    vsrc = t2 ? g4 * rowb + r * 16 : (lane >> 5) * 1024 + (lane & 31) * 16;
    sbase = t1 ? ks1 * 32 * (F * 2) + cq1 * 256 : t2 ? ks2 * 32 * (D * 2) + cq2 * 256 : nt * 16 * 1024 + hf * 512;
    sstep = t2 ? 4 * rowb : 2048;
    dbase = t1 ? WS_W1F + cq1 * 8 * W1_NT + ks1 * 1024 : t2 ? WS_W2F + cq2 * 8 * W2_NT + ks2 * 1024 : WS_WDF + nt * WD_NT + (im * 16 + hf * 8) * 1024;
    dstep = t1 ? W1_NT : t2 ? W2_NT : 1024;
    nfr = t1 ? (cq1 == 10 ? 6 : 8) : t2 ? (u2 < 172 ? 8 : 0) : (slot < 60 ? 8 : 0);
    if (w < 2) {
      usrc = pwm; ubytes = D * 6 * D * 2;
      vsrc = g4 * 8 * 6144 + r * 16; sbase = (xcd * 64 + w * 32) * 6144 + idx * 256; sstep = 6144;
      dbase = WS_MODP + (xcd * 2 + w) * 24576 + idx * 1024; nfr = idx < 24 ? 1 : 0; vdst = r * 64 + g4 * 16;
    }
    if (nfr == 0) sbase = OOB;
  }
  const v4i ssrc = make_srd(usrc, ubytes), sc = make_srd(pc, D * 2);
  v4u cv, fr[8], f0;
  const bool fm1 = (lane >> 1) != KB(0) && (lane >> 1) != KB(1), fm2 = (lane >> 5) != kh || ((lane >> 1) & 1) != kq;
#define LOADBV(i, k) LD4V_SC1I(Bf[i][k], l16, sws, WS_XNF + (KB(i) * 2 + ((k) >> 2)) * XN_KS + (8 * cj + 4 * nh) * 1024, ((k) & 3) * 1024)
  v4u Bf[8][8];
#define SIDE_JOB()                                                                                 \
  if (w >= 2) {                                                                                    \
    if (slot < 44) {                                                                               \
      const u32 a0 = scr + (8 * g4 + (r >> 2)) * 256 + (r & 3) * 8;                                \
      v2u t0[8], t1[8];                                                                            \
      _Pragma("unroll") for (int c = 0; c < 8; c++)                                                \
        asm volatile("ds_read_b64_tr_b16 %0, %2 offset:%3\n\tds_read_b64_tr_b16 %1, %2 offset:%4"  \
                     : "=&v"(t0[c]), "=&v"(t1[c]) : "v"(a0), "i"(c * 32), "i"(c * 32 + 1024) : "memory"); \
      WAITL(0);                                                                                    \
      _Pragma("unroll") for (int c = 0; c < 8; c++) {                                              \
        PINV(t0[c]); PINV(t1[c]);                                                                  \
        fr[c].x = t0[c].x; fr[c].y = t0[c].y; fr[c].z = t1[c].x; fr[c].w = t1[c].y;                \
      }                                                                                            \
    } else {                                                                                       \
      const u32 a0 = scr + r * 512 + g4 * 16;                                                      \
      _Pragma("unroll") for (int c = 0; c < 8; c++) fr[c] = lds_rd4(a0 + c * 64);                  \
    }                                                                                              \
  } else {                                                                                         \
    _Pragma("unroll") for (int c = 0; c < 8; c++) fr[c] = lds_rd4(scr + l16 + c * 1024);           \
    float acc[8];                                                                                  \
    _Pragma("unroll") for (int e = 0; e < 8; e++) acc[e] = 0.f;                                    \
    _Pragma("unroll") for (int i = 0; i < 8; i++) {                                                \
      const u32 cw = cv[i >> 1];                                                                   \
      const float cf = (i & 1) ? bfhi(cw) : bflo(cw);                                              \
      const float sl = rbf(cf * __builtin_amdgcn_rcpf(1.f + __expf(-cf)));                         \
      _Pragma("unroll") for (int e = 0; e < 4; e++) {                                              \
        acc[2 * e] += sl * bflo(fr[i][e]);                                                         \
        acc[2 * e + 1] += sl * bfhi(fr[i][e]);                                                     \
      }                                                                                            \
    }                                                                                              \
    float s4[4], s2[2];    \
    _Pragma("unroll") for (int e = 0; e < 4; e++) {                                                \
      const int q = (e >> 1) * 4 + (e & 1);                                                        \
      auto sw = __builtin_amdgcn_permlane16_swap(asu(acc[q]), asu(acc[q + 2]), false, false);      \
      s4[e] = asf((u32)sw[0]) + asf((u32)sw[1]);                                                   \
    }                                                                                              \
    _Pragma("unroll") for (int c = 0; c < 2; c++) {                                                \
      auto sw = __builtin_amdgcn_permlane32_swap(asu(s4[c]), asu(s4[2 + c]), false, false);        \
      s2[c] = asf((u32)sw[0]) + asf((u32)sw[1]);                                                   \
    }                                                                                              \
    fr[0].x = asu(s2[0]); fr[0].y = tag; fr[0].z = asu(s2[1]); fr[0].w = tag;                      \
  }
  WAITV(8);
  BARN();
  ST1_SC1(tag, lane < 8 ? lane * 1024 : OOB, sws, w == 0 ? WS_FLAG0R + rg * 4 : OOB);
  LD4V(cv, g4 * 16, sc, xcd * 128 + (w & 1) * 64);
  DMA(scr + 0, vsrc, ssrc, sbase + 0 * sstep);
  DMA(scr + 1024, vsrc, ssrc, sbase + 1 * sstep);
  DMA(scr + 2048, vsrc, ssrc, sbase + 2 * sstep);
  DMA(scr + 3072, vsrc, ssrc, sbase + 3 * sstep);
  DMA(scr + 4096, vsrc, ssrc, sbase + 4 * sstep);
  DMA(scr + 5120, vsrc, ssrc, sbase + 5 * sstep);
  DMA(scr + 6144, vsrc, ssrc, sbase + 6 * sstep);
  DMA(scr + 7168, vsrc, ssrc, sbase + 7 * sstep);
  u32 vA2 = voffA + KB(2) * 128; PINV(vA2);
  DMAI(ringw, 32768, vA2, sre, sR[0]);
  DMAI(ringw, 33792, vA2, sre, sR[1]);
  DMAI(ringw, 34816, vA2, sre, sR[2]);
  DMAI(ringw, 35840, vA2, sre, sR[3]);
  DMAI(ringw, 40960, vA2, sim, sR[0]);
  DMAI(ringw, 41984, vA2, sim, sR[1]);
  DMAI(ringw, 43008, vA2, sim, sR[2]);
  DMAI(ringw, 44032, vA2, sim, sR[3]);
  u32 vA3 = voffA + KB(3) * 128; PINV(vA3);
  DMAI(ringw, 49152, vA3, sre, sR[0]);
  DMAI(ringw, 50176, vA3, sre, sR[1]);
  DMAI(ringw, 51200, vA3, sre, sR[2]);
  DMAI(ringw, 52224, vA3, sre, sR[3]);
  DMAI(ringw, 57344, vA3, sim, sR[0]);
  DMAI(ringw, 58368, vA3, sim, sR[1]);
  DMAI(ringw, 59392, vA3, sim, sR[2]);
  DMAI(ringw, 60416, vA3, sim, sR[3]);
  LD4V_SC1(f0, l16, sws, WS_FLAG0R + xcd * 1024);
  WAITV(17);
  PINV(cv);
  SIDE_JOB();
  WAITV(0);
  PINV(f0);
  SPIN(!__all(fm2 || EQ4(f0)), POLL4(f0, l16, WS_FLAG0R + xcd * 1024));
  LOADBV(0, 0);
  LOADBV(0, 1);
  LOADBV(0, 2);
  LOADBV(0, 3);
  LOADBV(0, 4);
  LOADBV(0, 5);
  LOADBV(0, 6);
  LOADBV(0, 7);
  LOADBV(1, 0);
  LOADBV(1, 1);
  LOADBV(1, 2);
  LOADBV(1, 3);
  LOADBV(1, 4);
  LOADBV(1, 5);
  LOADBV(1, 6);
  LOADBV(1, 7);
  ST4_SC1(fr[0], vdst, sws, 0u < nfr ? dbase + 0 * dstep : OOB);
  ST4_SC1(fr[1], vdst, sws, 1u < nfr ? dbase + 1 * dstep : OOB);
  ST4_SC1(fr[2], vdst, sws, 2u < nfr ? dbase + 2 * dstep : OOB);
  ST4_SC1(fr[3], vdst, sws, 3u < nfr ? dbase + 3 * dstep : OOB);
  ST4_SC1(fr[4], vdst, sws, 4u < nfr ? dbase + 4 * dstep : OOB);
  ST4_SC1(fr[5], vdst, sws, 5u < nfr ? dbase + 5 * dstep : OOB);
  ST4_SC1(fr[6], vdst, sws, 6u < nfr ? dbase + 6 * dstep : OOB);
  ST4_SC1(fr[7], vdst, sws, 7u < nfr ? dbase + 7 * dstep : OOB);

  v4f acc[8][4], rs[2], a1[4][4], a2[4][3], a3[4][4], zz[2][4], cc[3];
  v4u ones = {0x3f803f80u, 0x3f803f80u, 0x3f803f80u, 0x3f803f80u};
  PINV(ones);
  const u32 rsw = lds0 + L_RSX + (g4 == 0 ? w * 128 + r * 4 : 512 + lane * 4);
  v4u Af[2][4], Ar, wd[32], mg[3], fw0, fw1, mp, pq, f4, sf[4], pa[32], zf[4], rf[4], gs[16], stt[4], A2[3][2];
  v4u w1f[48], w2f[44], ua[44];
  v2u mo, gr[4], gr2[4], xe[4], f2, x1p[4], xs[4], st2, op[4];
  v4f pr[3][4], gv[4], g2[4], x1[4], c1[3], c2[3], kb[2][3];
  v2f rm[4];
  v4f ut; v2u ux, uy;
  const u32 tid16 = tid * 16;
  const u32 mpv = (lane < 32 && w < 3) ? r * 24576 + g4 * 16 : OOB;
  const u32 mvo = (r == 0 && mpv != OOB) ? g4 * 8 : OOB;
  const u32 msec = (b * 3 + w) >> 7;
  const u32 mps = (b * 3 + w) * 32;
  u32 ard0 = ringq + r * 128 + ((g4 ^ (r & 7)) * 16), ard1 = ringq + r * 128 + (((4 + g4) ^ (r & 7)) * 16);
  PINV(ard0); PINV(ard1);
  const u32 ars = (nh ? ard1 : ard0) + ((cj - 2 * kq) & 3) * 2048;
  u32 xw = ringq + nh * 8192 + l16, xr = (ringq ^ 65536u) + nh * 8192 + l16;
  PINV(xw); PINV(xr);
  const u32 mcolA = lds0 + L_MOD + 1024 + (128 * cj + 64 * nh + 4 * g4) * 2;
  const u32 xev = r * 1024 + g4 * 8, xes = (tb * 64 + 16 * w) * 1024 + 128 * j;
  const u32 wdb = WS_WDF + 4 * j * WD_NT + 4 * w * 1024, sfb = lds0 + L_MOD + w * 256 + g4 * 16;
  const u32 rdb1 = lds0 + ((w - 1) & 3) * 12288 + l16, rdb2 = lds0 + ((w - 2) & 3) * 12288 + l16, rdb3 = lds0 + ((w - 3) & 3) * 12288 + l16;
  const u32 red = lds0 + w * 12288 + l16, red3 = lds0 + w * 16384 + l16, rd3 = lds0 + w * 4096 + l16;
  const u32 mcol = lds0 + L_MOD + (64 * j + 4 * g4) * 2;
  const u32 vfr = ((g4 & 1) * 2 + (g4 >> 1)) * 256 + r * 16;
  const u32 pqs = WS_PQF + tb * PQ_TB + (16 * kh + 4 * cj + 2 * nh) * PQ_KS + 2 * kq * 1024, pab = WS_PQF + tb * PQ_TB + 4 * w * PQ_KS;
  const u32 xco = WS_XCH + ((tb * 8 + j) * 4 + w) * 4096, xci = xco ^ 16384u;
  const u32 xfo = WS_XFL + ((tb * 8 + j) * 4 + w) * 128, xfi = xfo ^ 512u;
  const float sgn = kh ? -1.f : 1.f;
  v4u xp[4];
  const u32 fv8 = (lane & 7) * 128, fv2 = (2 * w + (lane & 1)) * 128, flg = WS_FLG + tb * 5120, flo = w == 0 ? flg + j * 128 : OOB;
  u32 f1, otv;
  u32 vrot[4], vrs[4];
#pragma unroll
  for (int mm = 0; mm < 4; mm++) {
    vrot[mm] = l16 + ((mm + w) & 3) * 1024;
    vrs[mm] = g4 < 2 ? ((mm + w) & 3) * 512 + l16 : OOB;
  }
  const u32 nt0 = (j < 7 ? 11 * j : 76) + ((j >= 6 && w == 3) ? 8 : 3 * w);
  const u32 ntn = (w == 3 || (j >= 6 && w == 2)) ? 2 : 3;
  const u32 w1b0 = WS_W1F + nt0 * W1_NT, w1b1 = w1b0 + W1_NT, w1b2 = ntn == 3 ? w1b0 + 2 * W1_NT : OOB;
  const u32 gsb = lds0 + L_MOD + 4096 - (r & 1) * 1024 + g4 * 16;
  const u32 k3 = 11 * w;
  const u32 w2b = WS_W2F + 4 * j * W2_NT + k3 * 1024, w2x = w == 3 ? OOB : w2b;
  const u32 uab = WS_UF + tb * U_TB + k3 * 4096, uax = w == 3 ? OOB : uab;
  const u32 a2w = lds0 + L_A2 + 16 * w * 1024, x1s = WS_X1F + tb * X1_TB + 16 * w * 1024;
  u32 a2r = lds0 + L_A2 + l16;
  const u32 vu = (g4 & 1) * 1024 + (g4 >> 1) * 256 + r * 16;
#define USOFF(nt) (WS_UF + tb * U_TB + ((nt) >> 1) * 4096 + ((nt) & 1) * 512)
  const u32 us0 = USOFF(nt0), us1 = USOFF(nt0 + 1), us2 = ntn == 3 ? USOFF(nt0 + 2) : OOB;
  const u32 vout = r * 1024 + (g4 & 1) * 32 + (g4 >> 1) * 16;
#define MOD_SUM()                                                                                  \
  do {                                                                                             \
    SPIN(!__all(mpv == OOB || (mp.y == tag && mp.w == tag)), POLL4(mp, mpv, WS_MODP + mps));      \
    float a = asf(mp.x), c = asf(mp.z);                                                            \
    a = dpp_add<0xB1>(a); c = dpp_add<0xB1>(c);                                                    \
    a = dpp_add<0x4E>(a); c = dpp_add<0x4E>(c);                                                    \
    a = dpp_add<0x141>(a); c = dpp_add<0x141>(c);                                                  \
    a = dpp_add<0x140>(a); c = dpp_add<0x140>(c);                                                  \
    a = rbf(a); c = rbf(c);                                                                        \
    if (msec == 1 || msec == 4) { a += 1.f; c += 1.f; }                                            \
    mo.x = pk2(a, c); mo.y = tag;                                                                  \
  } while (0)
#define MGOK(q) (mg[q].y == tag && mg[q].w == tag)
#define MG_RELOAD()                                                                                \
  asm volatile("buffer_load_dwordx4 %0, %3, %4, %5 offen sc1\n\tbuffer_load_dwordx4 %1, %3, %4, %6 offen sc1\n\t" \
               "buffer_load_dwordx4 %2, %3, %4, %7 offen sc1\n\ts_waitcnt vmcnt(0)"                \
               : "=&v"(mg[0]), "=&v"(mg[1]), "=&v"(mg[2])                                          \
               : "v"(tid16), "s"(sws), "s"(WS_MODG), "s"(WS_MODG + 4096), "s"(WS_MODG + 8192) : "memory")
#define MOD_STAGE()                                                                                \
  do {                                                                                             \
    SPIN(!__all(MGOK(0) && MGOK(1) && MGOK(2)), MG_RELOAD());                                      \
    _Pragma("unroll") for (int q = 0; q < 3; q++) {                                                \
      v2u o; o.x = mg[q].x; o.y = mg[q].z;                                                         \
      lds_wr2(lds0 + L_MOD + q * 2048 + tid * 8, o);                                               \
    }                                                                                              \
  } while (0)
#define UNPK(dst, src) do { dst = v4f{bflo(src.x), bfhi(src.x), bflo(src.y), bfhi(src.y)}; PINV(dst); } while (0)
#define PQ_TILE(X, m, n, x0, x1)                                                                   \
  do {                                                                                             \
    const v4f xo = {bflo(x0), bfhi(x0), bflo(x1), bfhi(x1)};                                       \
    const v4f t = ((acc[m][n] + pr[(m) & 1][n]) + xo) * gv[n];                                     \
    X.x = pk2(t.x, t.y); X.y = pk2(t.z, t.w);                                                      \
  } while (0)
#define PQ1(X, m, n) do { ut = acc[m][n] + pr[(m) & 1][n]; X.x = pk2(ut.x, ut.y); X.y = pk2(ut.z, ut.w); PINV(X); } while (0)
#define G1_EPI()                                                                                   \
  _Pragma("unroll") for (int n = 0; n < 4; n++) {                                                  \
    const v4f y = (a1[0][n] + pr[0][n]) + (pr[1][n] + pr[2][n]);                                   \
    const v4f xv = {bflo(xe[n].x), bfhi(xe[n].x), bflo(xe[n].y), bfhi(xe[n].y)};                   \
    const v4f t = xv + gv[n] * y;                                                                  \
    x1p[n].x = pk2(t.x, t.y); x1p[n].y = pk2(t.z, t.w);                                            \
    x1[n] = v4f{bflo(x1p[n].x), bfhi(x1p[n].x), bflo(x1p[n].y), bfhi(x1p[n].y)};                   \
    const v4f t2 = x1[n] * g2[n];                                                                  \
    xs[n].x = pk2(t2.x, t2.y); xs[n].y = pk2(t2.z, t2.w);                                          \
  }
#define G1_STATS()                                                                                 \
  do {                                                                                             \
    const v4f s4 = (x1[0] + x1[1]) + (x1[2] + x1[3]);                                              \
    const float mj = sum_rows((s4.x + s4.y) + (s4.z + s4.w)) * (1.f / 64.f);                       \
    const v4f d0 = x1[0] - mj, d1 = x1[1] - mj, d2 = x1[2] - mj, d3 = x1[3] - mj;                  \
    const v4f q4 = (d0 * d0 + d1 * d1) + (d2 * d2 + d3 * d3);                                      \
    st2.x = asu(mj); st2.y = asu(sum_rows((q4.x + q4.y) + (q4.z + q4.w)));                         \
  } while (0)
#define C12()                                                                                      \
  _Pragma("unroll") for (int nn = 0; nn < 3; nn++) {                                               \
    _Pragma("unroll") for (int e = 0; e < 4; e++) {                                                \
      c1[nn][e] = dpp_mov<0xA0>(cc[nn][e]);                                                        \
      c2[nn][e] = dpp_mov<0xF5>(cc[nn][e]);                                                        \
    }                                                                                              \
    PINV(c1[nn]); PINV(c2[nn]);                                                                    \
  }
  float rsa[4], rsm, rsq, rdd, rde;
#define ROWSTAT(st)                                                                                \
  do {                                                                                             \
    if ((st) == 0) { rsa[0] = asf(stt[0].x) + asf(stt[0].z); rsa[1] = asf(stt[1].x) + asf(stt[1].z); rsa[2] = asf(stt[2].x) + asf(stt[2].z); PINV(rsa[0]); PINV(rsa[1]); PINV(rsa[2]); } \
    if ((st) == 1) { rsa[3] = asf(stt[3].x) + asf(stt[3].z); rsa[0] += rsa[1]; rsa[2] += rsa[3]; PINV(rsa[0]); PINV(rsa[2]); } \
    if ((st) == 2) { rsm = (rsa[0] + rsa[2]) * 0.125f; rsa[0] = asf(stt[0].y) + asf(stt[0].w); PINV(rsm); PINV(rsa[0]); }      \
    if ((st) == 3) { rsa[1] = asf(stt[1].y) + asf(stt[1].w); rsa[2] = asf(stt[2].y) + asf(stt[2].w); rsa[3] = asf(stt[3].y) + asf(stt[3].w); PINV(rsa[1]); PINV(rsa[2]); PINV(rsa[3]); } \
    if ((st) == 4) { rsa[0] += rsa[1]; PINV(rsa[0]); rsa[2] += rsa[3]; PINV(rsa[2]); rsq = rsa[0] + rsa[2]; PINV(rsq); }       \
    if ((st) == 5) { float d0 = asf(stt[0].x) - rsm; PINV(d0); float d1 = asf(stt[0].z) - rsm; PINV(d1); rdd = d0 * d0; PINV(rdd); rde = d1 * d1; PINV(rde); } \
    if ((st) >= 6 && (st) <= 8) { float d0 = asf(stt[((st) - 5) & 3].x) - rsm; PINV(d0); float d1 = asf(stt[((st) - 5) & 3].z) - rsm; PINV(d1); rdd += d0 * d0; PINV(rdd); rde += d1 * d1; PINV(rde); } \
    if ((st) == 9) { rsq += 64.f * (rdd + rde); rsq = rsq * (1.f / D) + 1e-5f; PINV(rsq); }        \
    if ((st) == 10) { rsq = __builtin_amdgcn_rsqf(rsq); PINV(rsq); }                               \
    if ((st) == 11) { v2u o; o.x = asu(rsq); o.y = asu(rsm * rsq); lds_wr2(lds0 + L_PX + w * 512 + lane * 8, o); } \
  } while (0)
#define UT1(m, nn) do { ut = a2[m][nn] * rm[m].x + kb[(m) & 1][nn]; PINV(ut); } while (0)
#define UT2(X) do { X.x = relu_pk(pk2(ut.x, ut.y)); X.y = relu_pk(pk2(ut.z, ut.w)); PINV(X); } while (0)
#define ROWSTAT4() \
  _Pragma("unroll") for (int m = 0; m < 4; m++) rm[m] = *(const LAS v2f*)(lds0 + L_PX + w * 512 + r * 8 + m * 128)
#define U_TILE(X, m, nn)                                                                           \
  do {                                                                                             \
    const v4f t = a2[m][nn] * rm[m].x + kb[(m) & 1][nn];                                           \
    X.x = relu_pk(pk2(t.x, t.y)); X.y = relu_pk(pk2(t.z, t.w));                                    \
  } while (0)
#define G3_EPI()                                                                                   \
  _Pragma("unroll") for (int n = 0; n < 4; n++) {                                                  \
    const v4f vv = (p3[0][n] + p3[1][n]) + (p3[2][n] + p3[3][n]);                                  \
    const v4f xx = {bflo(x1p[n].x), bfhi(x1p[n].x), bflo(x1p[n].y), bfhi(x1p[n].y)};               \
    const v4f t = xx + gv[n] * vv;                                                                 \
    op[n].x = pk2(t.x, t.y); op[n].y = pk2(t.z, t.w);                                              \
  }

  BARN();
  Af[0][0] = lds_rd4(ard0 + 0);
  Af[0][1] = lds_rd4(ard0 + 2048);
  Af[0][2] = lds_rd4(ard0 + 4096);
  Af[0][3] = lds_rd4(ard0 + 6144);
  WAITV(20);
  PINV(Bf[0][0]);
  PINV(Bf[0][1]);
  PINV(Bf[0][2]);
  PINV(Bf[0][3]);
  Af[1][0] = lds_rd4(ard1 + 0);
  Af[1][1] = lds_rd4(ard1 + 2048);
  Af[1][2] = lds_rd4(ard1 + 4096);
  Af[1][3] = lds_rd4(ard1 + 6144);
  Ar = lds_rd4(ars + 0);
  SB();
  MF0(acc[0][0], "v", Bf[0][0], "v", Af[0][0]);
  MF0(acc[0][1], "v", Bf[0][1], "v", Af[0][0]);
  MF0(acc[0][2], "v", Bf[0][2], "v", Af[0][0]);
  MF0(acc[0][3], "v", Bf[0][3], "v", Af[0][0]);
  SB();
  SB();
  MF0(acc[1][0], "v", Bf[0][0], "v", Af[0][1]);
  MF0(acc[1][1], "v", Bf[0][1], "v", Af[0][1]);
  MF0(acc[1][2], "v", Bf[0][2], "v", Af[0][1]);
  MF0(acc[1][3], "v", Bf[0][3], "v", Af[0][1]);
  SB();
  SB();
  MF0(acc[2][0], "v", Bf[0][0], "v", Af[0][2]);
  MF0(acc[2][1], "v", Bf[0][1], "v", Af[0][2]);
  MF0(acc[2][2], "v", Bf[0][2], "v", Af[0][2]);
  MF0(acc[2][3], "v", Bf[0][3], "v", Af[0][2]);
  SB();
  SB();
  MF0(acc[3][0], "v", Bf[0][0], "v", Af[0][3]);
  MF0(acc[3][1], "v", Bf[0][1], "v", Af[0][3]);
  MF0(acc[3][2], "v", Bf[0][2], "v", Af[0][3]);
  MF0(acc[3][3], "v", Bf[0][3], "v", Af[0][3]);
  MF0(rs[0], "v", ones, "v", Ar);
  SB();
  SB();
  WAITV(16);
  PINV(Bf[0][4]);
  PINV(Bf[0][5]);
  PINV(Bf[0][6]);
  PINV(Bf[0][7]);
  Af[0][0] = lds_rd4(ard0 + 8192);
  Af[0][1] = lds_rd4(ard0 + 10240);
  Af[0][2] = lds_rd4(ard0 + 12288);
  Af[0][3] = lds_rd4(ard0 + 14336);
  SB();
  MF(acc[0][0], "v", Bf[0][4], "v", Af[1][0]);
  MF(acc[0][1], "v", Bf[0][5], "v", Af[1][0]);
  MF(acc[0][2], "v", Bf[0][6], "v", Af[1][0]);
  MF(acc[0][3], "v", Bf[0][7], "v", Af[1][0]);
  SB();
  SB();
  MF(acc[1][0], "v", Bf[0][4], "v", Af[1][1]);
  MF(acc[1][1], "v", Bf[0][5], "v", Af[1][1]);
  MF(acc[1][2], "v", Bf[0][6], "v", Af[1][1]);
  MF(acc[1][3], "v", Bf[0][7], "v", Af[1][1]);
  SB();
  SB();
  MF(acc[2][0], "v", Bf[0][4], "v", Af[1][2]);
  MF(acc[2][1], "v", Bf[0][5], "v", Af[1][2]);
  MF(acc[2][2], "v", Bf[0][6], "v", Af[1][2]);
  MF(acc[2][3], "v", Bf[0][7], "v", Af[1][2]);
  SB();
  SB();
  MF(acc[3][0], "v", Bf[0][4], "v", Af[1][3]);
  MF(acc[3][1], "v", Bf[0][5], "v", Af[1][3]);
  MF(acc[3][2], "v", Bf[0][6], "v", Af[1][3]);
  MF(acc[3][3], "v", Bf[0][7], "v", Af[1][3]);
  SB();
  SB();
  Af[1][0] = lds_rd4(ard1 + 8192);
  Af[1][1] = lds_rd4(ard1 + 10240);
  Af[1][2] = lds_rd4(ard1 + 12288);
  Af[1][3] = lds_rd4(ard1 + 14336);
  Ar = lds_rd4(ars + 8192);
  SB();
  MF0(acc[4][0], "v", Bf[0][0], "v", Af[0][0]);
  MF0(acc[4][1], "v", Bf[0][1], "v", Af[0][0]);
  MF0(acc[4][2], "v", Bf[0][2], "v", Af[0][0]);
  MF0(acc[4][3], "v", Bf[0][3], "v", Af[0][0]);
  SB();
  LOADBV(2, 0);
  LOADBV(2, 1);
  SB();
  MF0(acc[5][0], "v", Bf[0][0], "v", Af[0][1]);
  MF0(acc[5][1], "v", Bf[0][1], "v", Af[0][1]);
  MF0(acc[5][2], "v", Bf[0][2], "v", Af[0][1]);
  MF0(acc[5][3], "v", Bf[0][3], "v", Af[0][1]);
  SB();
  LOADBV(2, 2);
  SB();
  MF0(acc[6][0], "v", Bf[0][0], "v", Af[0][2]);
  MF0(acc[6][1], "v", Bf[0][1], "v", Af[0][2]);
  MF0(acc[6][2], "v", Bf[0][2], "v", Af[0][2]);
  MF0(acc[6][3], "v", Bf[0][3], "v", Af[0][2]);
  SB();
  LOADBV(2, 3);
  LOADBV(2, 4);
  SB();
  MF0(acc[7][0], "v", Bf[0][0], "v", Af[0][3]);
  MF0(acc[7][1], "v", Bf[0][1], "v", Af[0][3]);
  MF0(acc[7][2], "v", Bf[0][2], "v", Af[0][3]);
  MF0(acc[7][3], "v", Bf[0][3], "v", Af[0][3]);
  MF0(rs[1], "v", ones, "v", Ar);
  SB();
  LOADBV(2, 5);
  SB();
  BAR();
  Af[0][0] = lds_rd4(ard0 + 16384);
  Af[0][1] = lds_rd4(ard0 + 18432);
  Af[0][2] = lds_rd4(ard0 + 20480);
  Af[0][3] = lds_rd4(ard0 + 22528);
  SB();
  MF(acc[4][0], "v", Bf[0][4], "v", Af[1][0]);
  MF(acc[4][1], "v", Bf[0][5], "v", Af[1][0]);
  MF(acc[4][2], "v", Bf[0][6], "v", Af[1][0]);
  MF(acc[4][3], "v", Bf[0][7], "v", Af[1][0]);
  SB();
  LOADBV(2, 6);
  LOADBV(2, 7);
  SB();
  MF(acc[5][0], "v", Bf[0][4], "v", Af[1][1]);
  MF(acc[5][1], "v", Bf[0][5], "v", Af[1][1]);
  MF(acc[5][2], "v", Bf[0][6], "v", Af[1][1]);
  MF(acc[5][3], "v", Bf[0][7], "v", Af[1][1]);
  SB();
  u32 vA4 = voffA + KB(4) * 128; PINV(vA4);
  DMAI(ringw, 0, vA4, sre, sR[0]);
  SB();
  MF(acc[6][0], "v", Bf[0][4], "v", Af[1][2]);
  MF(acc[6][1], "v", Bf[0][5], "v", Af[1][2]);
  MF(acc[6][2], "v", Bf[0][6], "v", Af[1][2]);
  MF(acc[6][3], "v", Bf[0][7], "v", Af[1][2]);
  SB();
  DMAI(ringw, 1024, vA4, sre, sR[1]);
  DMAI(ringw, 2048, vA4, sre, sR[2]);
  SB();
  MF(acc[7][0], "v", Bf[0][4], "v", Af[1][3]);
  MF(acc[7][1], "v", Bf[0][5], "v", Af[1][3]);
  MF(acc[7][2], "v", Bf[0][6], "v", Af[1][3]);
  MF(acc[7][3], "v", Bf[0][7], "v", Af[1][3]);
  SB();
  DMAI(ringw, 3072, vA4, sre, sR[3]);
  SB();
  WAITV(12);
  ST1_SC1(tag, lane0, sws, w >= 2 ? WS_FLAGW + (b * 2 + w - 2) * 4 : OOB);
  PINV(Bf[1][0]);
  PINV(Bf[1][1]);
  PINV(Bf[1][2]);
  PINV(Bf[1][3]);
  Af[1][0] = lds_rd4(ard1 + 16384);
  Af[1][1] = lds_rd4(ard1 + 18432);
  Af[1][2] = lds_rd4(ard1 + 20480);
  Af[1][3] = lds_rd4(ard1 + 22528);
  Ar = lds_rd4(ars + 16384);
  SB();
  MF(acc[0][0], "v", Bf[1][0], "v", Af[0][0]);
  MF(acc[0][1], "v", Bf[1][1], "v", Af[0][0]);
  MF(acc[0][2], "v", Bf[1][2], "v", Af[0][0]);
  MF(acc[0][3], "v", Bf[1][3], "v", Af[0][0]);
  SB();
  u32 vA5 = voffA + KB(5) * 128; PINV(vA5);
  DMAI(ringw, 8192, vA5, sre, sR[0]);
  DMAI(ringw, 9216, vA5, sre, sR[1]);
  SB();
  MF(acc[1][0], "v", Bf[1][0], "v", Af[0][1]);
  MF(acc[1][1], "v", Bf[1][1], "v", Af[0][1]);
  MF(acc[1][2], "v", Bf[1][2], "v", Af[0][1]);
  MF(acc[1][3], "v", Bf[1][3], "v", Af[0][1]);
  SB();
  DMAI(ringw, 10240, vA5, sre, sR[2]);
  SB();
  MF(acc[2][0], "v", Bf[1][0], "v", Af[0][2]);
  MF(acc[2][1], "v", Bf[1][1], "v", Af[0][2]);
  MF(acc[2][2], "v", Bf[1][2], "v", Af[0][2]);
  MF(acc[2][3], "v", Bf[1][3], "v", Af[0][2]);
  SB();
  DMAI(ringw, 11264, vA5, sre, sR[3]);
  SB();
  MF(acc[3][0], "v", Bf[1][0], "v", Af[0][3]);
  MF(acc[3][1], "v", Bf[1][1], "v", Af[0][3]);
  MF(acc[3][2], "v", Bf[1][2], "v", Af[0][3]);
  MF(acc[3][3], "v", Bf[1][3], "v", Af[0][3]);
  MF(rs[0], "v", ones, "v", Ar);
  SB();
  SB();
  PINV(Bf[1][4]);
  PINV(Bf[1][5]);
  PINV(Bf[1][6]);
  PINV(Bf[1][7]);
  Af[0][0] = lds_rd4(ard0 + 24576);
  Af[0][1] = lds_rd4(ard0 + 26624);
  Af[0][2] = lds_rd4(ard0 + 28672);
  Af[0][3] = lds_rd4(ard0 + 30720);
  SB();
  MF(acc[0][0], "v", Bf[1][4], "v", Af[1][0]);
  MF(acc[0][1], "v", Bf[1][5], "v", Af[1][0]);
  MF(acc[0][2], "v", Bf[1][6], "v", Af[1][0]);
  MF(acc[0][3], "v", Bf[1][7], "v", Af[1][0]);
  SB();
  SB();
  MF(acc[1][0], "v", Bf[1][4], "v", Af[1][1]);
  MF(acc[1][1], "v", Bf[1][5], "v", Af[1][1]);
  MF(acc[1][2], "v", Bf[1][6], "v", Af[1][1]);
  MF(acc[1][3], "v", Bf[1][7], "v", Af[1][1]);
  SB();
  SB();
  MF(acc[2][0], "v", Bf[1][4], "v", Af[1][2]);
  MF(acc[2][1], "v", Bf[1][5], "v", Af[1][2]);
  MF(acc[2][2], "v", Bf[1][6], "v", Af[1][2]);
  MF(acc[2][3], "v", Bf[1][7], "v", Af[1][2]);
  SB();
  SB();
  MF(acc[3][0], "v", Bf[1][4], "v", Af[1][3]);
  MF(acc[3][1], "v", Bf[1][5], "v", Af[1][3]);
  MF(acc[3][2], "v", Bf[1][6], "v", Af[1][3]);
  MF(acc[3][3], "v", Bf[1][7], "v", Af[1][3]);
  SB();
  SB();
  Af[1][0] = lds_rd4(ard1 + 24576);
  Af[1][1] = lds_rd4(ard1 + 26624);
  Af[1][2] = lds_rd4(ard1 + 28672);
  Af[1][3] = lds_rd4(ard1 + 30720);
  Ar = lds_rd4(ars + 24576);
  SB();
  MF(acc[4][0], "v", Bf[1][0], "v", Af[0][0]);
  MF(acc[4][1], "v", Bf[1][1], "v", Af[0][0]);
  MF(acc[4][2], "v", Bf[1][2], "v", Af[0][0]);
  MF(acc[4][3], "v", Bf[1][3], "v", Af[0][0]);
  SB();
  LOADBV(3, 0);
  LOADBV(3, 1);
  SB();
  MF(acc[5][0], "v", Bf[1][0], "v", Af[0][1]);
  MF(acc[5][1], "v", Bf[1][1], "v", Af[0][1]);
  MF(acc[5][2], "v", Bf[1][2], "v", Af[0][1]);
  MF(acc[5][3], "v", Bf[1][3], "v", Af[0][1]);
  SB();
  LOADBV(3, 2);
  SB();
  MF(acc[6][0], "v", Bf[1][0], "v", Af[0][2]);
  MF(acc[6][1], "v", Bf[1][1], "v", Af[0][2]);
  MF(acc[6][2], "v", Bf[1][2], "v", Af[0][2]);
  MF(acc[6][3], "v", Bf[1][3], "v", Af[0][2]);
  SB();
  LOADBV(3, 3);
  LOADBV(3, 4);
  SB();
  MF(acc[7][0], "v", Bf[1][0], "v", Af[0][3]);
  MF(acc[7][1], "v", Bf[1][1], "v", Af[0][3]);
  MF(acc[7][2], "v", Bf[1][2], "v", Af[0][3]);
  MF(acc[7][3], "v", Bf[1][3], "v", Af[0][3]);
  MF(rs[1], "v", ones, "v", Ar);
  SB();
  LOADBV(3, 5);
  SB();
  LD4V_SC1(mp, mpv, sws, WS_MODP + mps);
  BAR();
  Af[0][0] = lds_rd4(ard0 + 32768);
  Af[0][1] = lds_rd4(ard0 + 34816);
  Af[0][2] = lds_rd4(ard0 + 36864);
  Af[0][3] = lds_rd4(ard0 + 38912);
  SB();
  MF(acc[4][0], "v", Bf[1][4], "v", Af[1][0]);
  MF(acc[4][1], "v", Bf[1][5], "v", Af[1][0]);
  MF(acc[4][2], "v", Bf[1][6], "v", Af[1][0]);
  MF(acc[4][3], "v", Bf[1][7], "v", Af[1][0]);
  SB();
  LOADBV(3, 6);
  LOADBV(3, 7);
  SB();
  MF(acc[5][0], "v", Bf[1][4], "v", Af[1][1]);
  MF(acc[5][1], "v", Bf[1][5], "v", Af[1][1]);
  MF(acc[5][2], "v", Bf[1][6], "v", Af[1][1]);
  MF(acc[5][3], "v", Bf[1][7], "v", Af[1][1]);
  SB();
  u32 vA6 = voffA + KB(6) * 128; PINV(vA6);
  DMAI(ringw, 16384, vA6, sre, sR[0]);
  SB();
  MF(acc[6][0], "v", Bf[1][4], "v", Af[1][2]);
  MF(acc[6][1], "v", Bf[1][5], "v", Af[1][2]);
  MF(acc[6][2], "v", Bf[1][6], "v", Af[1][2]);
  MF(acc[6][3], "v", Bf[1][7], "v", Af[1][2]);
  SB();
  DMAI(ringw, 17408, vA6, sre, sR[1]);
  DMAI(ringw, 18432, vA6, sre, sR[2]);
  SB();
  MF(acc[7][0], "v", Bf[1][4], "v", Af[1][3]);
  MF(acc[7][1], "v", Bf[1][5], "v", Af[1][3]);
  MF(acc[7][2], "v", Bf[1][6], "v", Af[1][3]);
  MF(acc[7][3], "v", Bf[1][7], "v", Af[1][3]);
  SB();
  DMAI(ringw, 19456, vA6, sre, sR[3]);
  SB();
  WAITV(26);
  PINV(Bf[2][0]);
  PINV(Bf[2][1]);
  PINV(Bf[2][2]);
  PINV(Bf[2][3]);
  Af[1][0] = lds_rd4(ard1 + 32768);
  Af[1][1] = lds_rd4(ard1 + 34816);
  Af[1][2] = lds_rd4(ard1 + 36864);
  Af[1][3] = lds_rd4(ard1 + 38912);
  Ar = lds_rd4(ars + 32768);
  SB();
  MF(acc[0][0], "v", Bf[2][0], "v", Af[0][0]);
  MF(acc[0][1], "v", Bf[2][1], "v", Af[0][0]);
  MF(acc[0][2], "v", Bf[2][2], "v", Af[0][0]);
  MF(acc[0][3], "v", Bf[2][3], "v", Af[0][0]);
  SB();
  u32 vA7 = voffA + KB(7) * 128; PINV(vA7);
  DMAI(ringw, 24576, vA7, sre, sR[0]);
  DMAI(ringw, 25600, vA7, sre, sR[1]);
  SB();
  MF(acc[1][0], "v", Bf[2][0], "v", Af[0][1]);
  MF(acc[1][1], "v", Bf[2][1], "v", Af[0][1]);
  MF(acc[1][2], "v", Bf[2][2], "v", Af[0][1]);
  MF(acc[1][3], "v", Bf[2][3], "v", Af[0][1]);
  SB();
  DMAI(ringw, 26624, vA7, sre, sR[2]);
  SB();
  MF(acc[2][0], "v", Bf[2][0], "v", Af[0][2]);
  MF(acc[2][1], "v", Bf[2][1], "v", Af[0][2]);
  MF(acc[2][2], "v", Bf[2][2], "v", Af[0][2]);
  MF(acc[2][3], "v", Bf[2][3], "v", Af[0][2]);
  SB();
  DMAI(ringw, 27648, vA7, sre, sR[3]);
  SB();
  MF(acc[3][0], "v", Bf[2][0], "v", Af[0][3]);
  MF(acc[3][1], "v", Bf[2][1], "v", Af[0][3]);
  MF(acc[3][2], "v", Bf[2][2], "v", Af[0][3]);
  MF(acc[3][3], "v", Bf[2][3], "v", Af[0][3]);
  MF(rs[0], "v", ones, "v", Ar);
  SB();
  SB();
  WAITV(26);
  PINV(Bf[2][4]);
  PINV(Bf[2][5]);
  PINV(Bf[2][6]);
  PINV(Bf[2][7]);
  Af[0][0] = lds_rd4(ard0 + 40960);
  Af[0][1] = lds_rd4(ard0 + 43008);
  Af[0][2] = lds_rd4(ard0 + 45056);
  Af[0][3] = lds_rd4(ard0 + 47104);
  SB();
  MF(acc[0][0], "v", Bf[2][4], "v", Af[1][0]);
  MF(acc[0][1], "v", Bf[2][5], "v", Af[1][0]);
  MF(acc[0][2], "v", Bf[2][6], "v", Af[1][0]);
  MF(acc[0][3], "v", Bf[2][7], "v", Af[1][0]);
  SB();
  SB();
  MF(acc[1][0], "v", Bf[2][4], "v", Af[1][1]);
  MF(acc[1][1], "v", Bf[2][5], "v", Af[1][1]);
  MF(acc[1][2], "v", Bf[2][6], "v", Af[1][1]);
  MF(acc[1][3], "v", Bf[2][7], "v", Af[1][1]);
  SB();
  SB();
  MF(acc[2][0], "v", Bf[2][4], "v", Af[1][2]);
  MF(acc[2][1], "v", Bf[2][5], "v", Af[1][2]);
  MF(acc[2][2], "v", Bf[2][6], "v", Af[1][2]);
  MF(acc[2][3], "v", Bf[2][7], "v", Af[1][2]);
  SB();
  SB();
  MF(acc[3][0], "v", Bf[2][4], "v", Af[1][3]);
  MF(acc[3][1], "v", Bf[2][5], "v", Af[1][3]);
  MF(acc[3][2], "v", Bf[2][6], "v", Af[1][3]);
  MF(acc[3][3], "v", Bf[2][7], "v", Af[1][3]);
  SB();
  SB();
  Af[1][0] = lds_rd4(ard1 + 40960);
  Af[1][1] = lds_rd4(ard1 + 43008);
  Af[1][2] = lds_rd4(ard1 + 45056);
  Af[1][3] = lds_rd4(ard1 + 47104);
  Ar = lds_rd4(ars + 40960);
  SB();
  MF(acc[4][0], "v", Bf[2][0], "v", Af[0][0]);
  MF(acc[4][1], "v", Bf[2][1], "v", Af[0][0]);
  MF(acc[4][2], "v", Bf[2][2], "v", Af[0][0]);
  MF(acc[4][3], "v", Bf[2][3], "v", Af[0][0]);
  SB();
  LOADBV(4, 0);
  LOADBV(4, 1);
  SB();
  MF(acc[5][0], "v", Bf[2][0], "v", Af[0][1]);
  MF(acc[5][1], "v", Bf[2][1], "v", Af[0][1]);
  MF(acc[5][2], "v", Bf[2][2], "v", Af[0][1]);
  MF(acc[5][3], "v", Bf[2][3], "v", Af[0][1]);
  SB();
  LOADBV(4, 2);
  SB();
  MF(acc[6][0], "v", Bf[2][0], "v", Af[0][2]);
  MF(acc[6][1], "v", Bf[2][1], "v", Af[0][2]);
  MF(acc[6][2], "v", Bf[2][2], "v", Af[0][2]);
  MF(acc[6][3], "v", Bf[2][3], "v", Af[0][2]);
  SB();
  LOADBV(4, 3);
  LOADBV(4, 4);
  SB();
  MF(acc[7][0], "v", Bf[2][0], "v", Af[0][3]);
  MF(acc[7][1], "v", Bf[2][1], "v", Af[0][3]);
  MF(acc[7][2], "v", Bf[2][2], "v", Af[0][3]);
  MF(acc[7][3], "v", Bf[2][3], "v", Af[0][3]);
  MF(rs[1], "v", ones, "v", Ar);
  SB();
  LOADBV(4, 5);
  SB();
  WAITV(16);
  PINV(mp);
  MOD_SUM();
  ST2_SC1(mo, mvo, sws, WS_MODG + (mps >> 1));
  LD4V_SC1(fw0, l16, sws, WS_FLAGW);
  LD4V_SC1(fw1, l16, sws, WS_FLAGW + 1024);
  BAR();
  Af[0][0] = lds_rd4(ard0 + 49152);
  Af[0][1] = lds_rd4(ard0 + 51200);
  Af[0][2] = lds_rd4(ard0 + 53248);
  Af[0][3] = lds_rd4(ard0 + 55296);
  SB();
  MF(acc[4][0], "v", Bf[2][4], "v", Af[1][0]);
  MF(acc[4][1], "v", Bf[2][5], "v", Af[1][0]);
  MF(acc[4][2], "v", Bf[2][6], "v", Af[1][0]);
  MF(acc[4][3], "v", Bf[2][7], "v", Af[1][0]);
  SB();
  LOADBV(4, 6);
  LOADBV(4, 7);
  SB();
  MF(acc[5][0], "v", Bf[2][4], "v", Af[1][1]);
  MF(acc[5][1], "v", Bf[2][5], "v", Af[1][1]);
  MF(acc[5][2], "v", Bf[2][6], "v", Af[1][1]);
  MF(acc[5][3], "v", Bf[2][7], "v", Af[1][1]);
  SB();
  DMAI(ringw, 32768, vA4, sim, sR[0]);
  SB();
  MF(acc[6][0], "v", Bf[2][4], "v", Af[1][2]);
  MF(acc[6][1], "v", Bf[2][5], "v", Af[1][2]);
  MF(acc[6][2], "v", Bf[2][6], "v", Af[1][2]);
  MF(acc[6][3], "v", Bf[2][7], "v", Af[1][2]);
  SB();
  DMAI(ringw, 33792, vA4, sim, sR[1]);
  DMAI(ringw, 34816, vA4, sim, sR[2]);
  SB();
  MF(acc[7][0], "v", Bf[2][4], "v", Af[1][3]);
  MF(acc[7][1], "v", Bf[2][5], "v", Af[1][3]);
  MF(acc[7][2], "v", Bf[2][6], "v", Af[1][3]);
  MF(acc[7][3], "v", Bf[2][7], "v", Af[1][3]);
  SB();
  DMAI(ringw, 35840, vA4, sim, sR[3]);
  SB();
  PINV(Bf[3][0]);
  PINV(Bf[3][1]);
  PINV(Bf[3][2]);
  PINV(Bf[3][3]);
  Af[1][0] = lds_rd4(ard1 + 49152);
  Af[1][1] = lds_rd4(ard1 + 51200);
  Af[1][2] = lds_rd4(ard1 + 53248);
  Af[1][3] = lds_rd4(ard1 + 55296);
  Ar = lds_rd4(ars + 49152);
  SB();
  MF(acc[0][0], "v", Bf[3][0], "v", Af[0][0]);
  MF(acc[0][1], "v", Bf[3][1], "v", Af[0][0]);
  MF(acc[0][2], "v", Bf[3][2], "v", Af[0][0]);
  MF(acc[0][3], "v", Bf[3][3], "v", Af[0][0]);
  SB();
  LOADBV(5, 0);
  LOADBV(5, 1);
  SB();
  MF(acc[1][0], "v", Bf[3][0], "v", Af[0][1]);
  MF(acc[1][1], "v", Bf[3][1], "v", Af[0][1]);
  MF(acc[1][2], "v", Bf[3][2], "v", Af[0][1]);
  MF(acc[1][3], "v", Bf[3][3], "v", Af[0][1]);
  SB();
  DMAI(ringw, 40960, vA5, sim, sR[0]);
  SB();
  MF(acc[2][0], "v", Bf[3][0], "v", Af[0][2]);
  MF(acc[2][1], "v", Bf[3][1], "v", Af[0][2]);
  MF(acc[2][2], "v", Bf[3][2], "v", Af[0][2]);
  MF(acc[2][3], "v", Bf[3][3], "v", Af[0][2]);
  SB();
  LOADBV(5, 2);
  LOADBV(5, 3);
  SB();
  MF(acc[3][0], "v", Bf[3][0], "v", Af[0][3]);
  MF(acc[3][1], "v", Bf[3][1], "v", Af[0][3]);
  MF(acc[3][2], "v", Bf[3][2], "v", Af[0][3]);
  MF(acc[3][3], "v", Bf[3][3], "v", Af[0][3]);
  MF(rs[0], "v", ones, "v", Ar);
  SB();
  DMAI(ringw, 41984, vA5, sim, sR[1]);
  SB();
  LD4V(cpf, l16, scode, (w == 3 && idx >= 9) ? idx * 1024 : OOB);
  WAITV(30);
  PINV(Bf[3][4]);
  PINV(Bf[3][5]);
  PINV(Bf[3][6]);
  PINV(Bf[3][7]);
  WAITV(13);
  PINV(fw0);
  PINV(fw1);
  SPIN(!__all(EQ4(fw0) && EQ4(fw1)), POLL4(fw0, l16, WS_FLAGW); POLL4(fw1, l16, WS_FLAGW + 1024));
  ST1_SC1(tag, lane0, sws, (b == 0 && w == 0) ? WS_E : OOB);
  Af[0][0] = lds_rd4(ard0 + 57344);
  Af[0][1] = lds_rd4(ard0 + 59392);
  Af[0][2] = lds_rd4(ard0 + 61440);
  Af[0][3] = lds_rd4(ard0 + 63488);
  SB();
  MF(acc[0][0], "v", Bf[3][4], "v", Af[1][0]);
  MF(acc[0][1], "v", Bf[3][5], "v", Af[1][0]);
  MF(acc[0][2], "v", Bf[3][6], "v", Af[1][0]);
  MF(acc[0][3], "v", Bf[3][7], "v", Af[1][0]);
  SB();
  LOADBV(5, 4);
  LOADBV(5, 5);
  SB();
  MF(acc[1][0], "v", Bf[3][4], "v", Af[1][1]);
  MF(acc[1][1], "v", Bf[3][5], "v", Af[1][1]);
  MF(acc[1][2], "v", Bf[3][6], "v", Af[1][1]);
  MF(acc[1][3], "v", Bf[3][7], "v", Af[1][1]);
  SB();
  DMAI(ringw, 43008, vA5, sim, sR[2]);
  SB();
  MF(acc[2][0], "v", Bf[3][4], "v", Af[1][2]);
  MF(acc[2][1], "v", Bf[3][5], "v", Af[1][2]);
  MF(acc[2][2], "v", Bf[3][6], "v", Af[1][2]);
  MF(acc[2][3], "v", Bf[3][7], "v", Af[1][2]);
  SB();
  LOADBV(5, 6);
  LOADBV(5, 7);
  SB();
  MF(acc[3][0], "v", Bf[3][4], "v", Af[1][3]);
  MF(acc[3][1], "v", Bf[3][5], "v", Af[1][3]);
  MF(acc[3][2], "v", Bf[3][6], "v", Af[1][3]);
  MF(acc[3][3], "v", Bf[3][7], "v", Af[1][3]);
  SB();
  DMAI(ringw, 44032, vA5, sim, sR[3]);
  SB();
  Af[1][0] = lds_rd4(ard1 + 57344);
  Af[1][1] = lds_rd4(ard1 + 59392);
  Af[1][2] = lds_rd4(ard1 + 61440);
  Af[1][3] = lds_rd4(ard1 + 63488);
  Ar = lds_rd4(ars + 57344);
  SB();
  MF(acc[4][0], "v", Bf[3][0], "v", Af[0][0]);
  MF(acc[4][1], "v", Bf[3][1], "v", Af[0][0]);
  MF(acc[4][2], "v", Bf[3][2], "v", Af[0][0]);
  MF(acc[4][3], "v", Bf[3][3], "v", Af[0][0]);
  SB();
  LOADBV(6, 0);
  LOADBV(6, 1);
  SB();
  MF(acc[5][0], "v", Bf[3][0], "v", Af[0][1]);
  MF(acc[5][1], "v", Bf[3][1], "v", Af[0][1]);
  MF(acc[5][2], "v", Bf[3][2], "v", Af[0][1]);
  MF(acc[5][3], "v", Bf[3][3], "v", Af[0][1]);
  SB();
  LOADBV(6, 2);
  SB();
  MF(acc[6][0], "v", Bf[3][0], "v", Af[0][2]);
  MF(acc[6][1], "v", Bf[3][1], "v", Af[0][2]);
  MF(acc[6][2], "v", Bf[3][2], "v", Af[0][2]);
  MF(acc[6][3], "v", Bf[3][3], "v", Af[0][2]);
  SB();
  LOADBV(6, 3);
  LOADBV(6, 4);
  SB();
  MF(acc[7][0], "v", Bf[3][0], "v", Af[0][3]);
  MF(acc[7][1], "v", Bf[3][1], "v", Af[0][3]);
  MF(acc[7][2], "v", Bf[3][2], "v", Af[0][3]);
  MF(acc[7][3], "v", Bf[3][3], "v", Af[0][3]);
  MF(rs[1], "v", ones, "v", Ar);
  SB();
  LOADBV(6, 5);
  SB();
  BAR();
  Af[0][0] = lds_rd4(ard0 + 0);
  Af[0][1] = lds_rd4(ard0 + 2048);
  Af[0][2] = lds_rd4(ard0 + 4096);
  Af[0][3] = lds_rd4(ard0 + 6144);
  SB();
  MF(acc[4][0], "v", Bf[3][4], "v", Af[1][0]);
  MF(acc[4][1], "v", Bf[3][5], "v", Af[1][0]);
  MF(acc[4][2], "v", Bf[3][6], "v", Af[1][0]);
  MF(acc[4][3], "v", Bf[3][7], "v", Af[1][0]);
  SB();
  LOADBV(6, 6);
  LOADBV(6, 7);
  SB();
  MF(acc[5][0], "v", Bf[3][4], "v", Af[1][1]);
  MF(acc[5][1], "v", Bf[3][5], "v", Af[1][1]);
  MF(acc[5][2], "v", Bf[3][6], "v", Af[1][1]);
  MF(acc[5][3], "v", Bf[3][7], "v", Af[1][1]);
  SB();
  DMAI(ringw, 49152, vA6, sim, sR[0]);
  SB();
  MF(acc[6][0], "v", Bf[3][4], "v", Af[1][2]);
  MF(acc[6][1], "v", Bf[3][5], "v", Af[1][2]);
  MF(acc[6][2], "v", Bf[3][6], "v", Af[1][2]);
  MF(acc[6][3], "v", Bf[3][7], "v", Af[1][2]);
  SB();
  DMAI(ringw, 50176, vA6, sim, sR[1]);
  DMAI(ringw, 51200, vA6, sim, sR[2]);
  SB();
  MF(acc[7][0], "v", Bf[3][4], "v", Af[1][3]);
  MF(acc[7][1], "v", Bf[3][5], "v", Af[1][3]);
  MF(acc[7][2], "v", Bf[3][6], "v", Af[1][3]);
  MF(acc[7][3], "v", Bf[3][7], "v", Af[1][3]);
  SB();
  DMAI(ringw, 52224, vA6, sim, sR[3]);
  SB();
  PINV(Bf[4][0]);
  PINV(Bf[4][1]);
  PINV(Bf[4][2]);
  PINV(Bf[4][3]);
  Af[1][0] = lds_rd4(ard1 + 0);
  Af[1][1] = lds_rd4(ard1 + 2048);
  Af[1][2] = lds_rd4(ard1 + 4096);
  Af[1][3] = lds_rd4(ard1 + 6144);
  Ar = lds_rd4(ars + 0);
  SB();
  MF(acc[0][0], "v", Bf[4][0], "v", Af[0][0]);
  MF(acc[0][1], "v", Bf[4][1], "v", Af[0][0]);
  MF(acc[0][2], "v", Bf[4][2], "v", Af[0][0]);
  MF(acc[0][3], "v", Bf[4][3], "v", Af[0][0]);
  SB();
  LOADBV(7, 0);
  LOADBV(7, 1);
  SB();
  MF(acc[1][0], "v", Bf[4][0], "v", Af[0][1]);
  MF(acc[1][1], "v", Bf[4][1], "v", Af[0][1]);
  MF(acc[1][2], "v", Bf[4][2], "v", Af[0][1]);
  MF(acc[1][3], "v", Bf[4][3], "v", Af[0][1]);
  SB();
  DMAI(ringw, 57344, vA7, sim, sR[0]);
  SB();
  MF(acc[2][0], "v", Bf[4][0], "v", Af[0][2]);
  MF(acc[2][1], "v", Bf[4][1], "v", Af[0][2]);
  MF(acc[2][2], "v", Bf[4][2], "v", Af[0][2]);
  MF(acc[2][3], "v", Bf[4][3], "v", Af[0][2]);
  SB();
  LOADBV(7, 2);
  LOADBV(7, 3);
  SB();
  MF(acc[3][0], "v", Bf[4][0], "v", Af[0][3]);
  MF(acc[3][1], "v", Bf[4][1], "v", Af[0][3]);
  MF(acc[3][2], "v", Bf[4][2], "v", Af[0][3]);
  MF(acc[3][3], "v", Bf[4][3], "v", Af[0][3]);
  MF(rs[0], "v", ones, "v", Ar);
  SB();
  DMAI(ringw, 58368, vA7, sim, sR[1]);
  SB();
  WAITV(36);
  PINV(Bf[4][4]);
  PINV(Bf[4][5]);
  PINV(Bf[4][6]);
  PINV(Bf[4][7]);
  Af[0][0] = lds_rd4(ard0 + 8192);
  Af[0][1] = lds_rd4(ard0 + 10240);
  Af[0][2] = lds_rd4(ard0 + 12288);
  Af[0][3] = lds_rd4(ard0 + 14336);
  SB();
  MF(acc[0][0], "v", Bf[4][4], "v", Af[1][0]);
  MF(acc[0][1], "v", Bf[4][5], "v", Af[1][0]);
  MF(acc[0][2], "v", Bf[4][6], "v", Af[1][0]);
  MF(acc[0][3], "v", Bf[4][7], "v", Af[1][0]);
  SB();
  LOADBV(7, 4);
  LOADBV(7, 5);
  SB();
  MF(acc[1][0], "v", Bf[4][4], "v", Af[1][1]);
  MF(acc[1][1], "v", Bf[4][5], "v", Af[1][1]);
  MF(acc[1][2], "v", Bf[4][6], "v", Af[1][1]);
  MF(acc[1][3], "v", Bf[4][7], "v", Af[1][1]);
  SB();
  DMAI(ringw, 59392, vA7, sim, sR[2]);
  SB();
  MF(acc[2][0], "v", Bf[4][4], "v", Af[1][2]);
  MF(acc[2][1], "v", Bf[4][5], "v", Af[1][2]);
  MF(acc[2][2], "v", Bf[4][6], "v", Af[1][2]);
  MF(acc[2][3], "v", Bf[4][7], "v", Af[1][2]);
  SB();
  LOADBV(7, 6);
  LOADBV(7, 7);
  SB();
  MF(acc[3][0], "v", Bf[4][4], "v", Af[1][3]);
  MF(acc[3][1], "v", Bf[4][5], "v", Af[1][3]);
  MF(acc[3][2], "v", Bf[4][6], "v", Af[1][3]);
  MF(acc[3][3], "v", Bf[4][7], "v", Af[1][3]);
  SB();
  DMAI(ringw, 60416, vA7, sim, sR[3]);
  SB();
  WAITV(33);
  PINV(Bf[5][0]);
  PINV(Bf[5][1]);
  PINV(Bf[5][2]);
  PINV(Bf[5][3]);
  Af[1][0] = lds_rd4(ard1 + 8192);
  Af[1][1] = lds_rd4(ard1 + 10240);
  Af[1][2] = lds_rd4(ard1 + 12288);
  Af[1][3] = lds_rd4(ard1 + 14336);
  Ar = lds_rd4(ars + 8192);
  SB();
  MF(acc[0][0], "v", Bf[5][0], "v", Af[0][0]);
  MF(acc[0][1], "v", Bf[5][1], "v", Af[0][0]);
  MF(acc[0][2], "v", Bf[5][2], "v", Af[0][0]);
  MF(acc[0][3], "v", Bf[5][3], "v", Af[0][0]);
  SB();
  LD4A_SC1I(wd[0], l16, sws, wdb + 0 * WD_NT + 0, 0);
  LD4A_SC1I(wd[4], l16, sws, wdb + 0 * WD_NT + 0, 1024);
  SB();
  MF(acc[1][0], "v", Bf[5][0], "v", Af[0][1]);
  MF(acc[1][1], "v", Bf[5][1], "v", Af[0][1]);
  MF(acc[1][2], "v", Bf[5][2], "v", Af[0][1]);
  MF(acc[1][3], "v", Bf[5][3], "v", Af[0][1]);
  SB();
  LD4A_SC1I(wd[1], l16, sws, wdb + 1 * WD_NT + 0, 0);
  SB();
  MF(acc[2][0], "v", Bf[5][0], "v", Af[0][2]);
  MF(acc[2][1], "v", Bf[5][1], "v", Af[0][2]);
  MF(acc[2][2], "v", Bf[5][2], "v", Af[0][2]);
  MF(acc[2][3], "v", Bf[5][3], "v", Af[0][2]);
  SB();
  LD4A_SC1I(wd[5], l16, sws, wdb + 1 * WD_NT + 0, 1024);
  LD4A_SC1I(wd[2], l16, sws, wdb + 2 * WD_NT + 0, 0);
  SB();
  MF(acc[3][0], "v", Bf[5][0], "v", Af[0][3]);
  MF(acc[3][1], "v", Bf[5][1], "v", Af[0][3]);
  MF(acc[3][2], "v", Bf[5][2], "v", Af[0][3]);
  MF(acc[3][3], "v", Bf[5][3], "v", Af[0][3]);
  MF(rs[0], "v", ones, "v", Ar);
  SB();
  LD4A_SC1I(wd[6], l16, sws, wdb + 2 * WD_NT + 0, 1024);
  SB();
  WAITV(31);
  PINV(Bf[5][4]);
  PINV(Bf[5][5]);
  PINV(Bf[5][6]);
  PINV(Bf[5][7]);
  BAR();
  Af[0][0] = lds_rd4(ard0 + 16384);
  Af[0][1] = lds_rd4(ard0 + 18432);
  Af[0][2] = lds_rd4(ard0 + 20480);
  Af[0][3] = lds_rd4(ard0 + 22528);
  SB();
  MF(acc[0][0], "v", Bf[5][4], "v", Af[1][0]);
  MF(acc[0][1], "v", Bf[5][5], "v", Af[1][0]);
  MF(acc[0][2], "v", Bf[5][6], "v", Af[1][0]);
  MF(acc[0][3], "v", Bf[5][7], "v", Af[1][0]);
  SB();
  LD4A_SC1I(wd[3], l16, sws, wdb + 3 * WD_NT + 0, 0);
  LD4A_SC1I(wd[7], l16, sws, wdb + 3 * WD_NT + 0, 1024);
  SB();
  MF(acc[1][0], "v", Bf[5][4], "v", Af[1][1]);
  MF(acc[1][1], "v", Bf[5][5], "v", Af[1][1]);
  MF(acc[1][2], "v", Bf[5][6], "v", Af[1][1]);
  MF(acc[1][3], "v", Bf[5][7], "v", Af[1][1]);
  SB();
  LD4A_SC1I(wd[8], l16, sws, wdb + 0 * WD_NT + 0, 2048);
  SB();
  MF(acc[2][0], "v", Bf[5][4], "v", Af[1][2]);
  MF(acc[2][1], "v", Bf[5][5], "v", Af[1][2]);
  MF(acc[2][2], "v", Bf[5][6], "v", Af[1][2]);
  MF(acc[2][3], "v", Bf[5][7], "v", Af[1][2]);
  SB();
  LD4A_SC1I(wd[12], l16, sws, wdb + 0 * WD_NT + 0, 3072);
  LD4V_SC1(mg[0], tid16, sws, WS_MODG + 0);
  SB();
  MF(acc[3][0], "v", Bf[5][4], "v", Af[1][3]);
  MF(acc[3][1], "v", Bf[5][5], "v", Af[1][3]);
  MF(acc[3][2], "v", Bf[5][6], "v", Af[1][3]);
  MF(acc[3][3], "v", Bf[5][7], "v", Af[1][3]);
  SB();
  LD4V_SC1(mg[1], tid16, sws, WS_MODG + 4096);
  SB();
  WAITV(32);
  PINV(Bf[6][0]);
  PINV(Bf[6][1]);
  PINV(Bf[6][2]);
  PINV(Bf[6][3]);
  Af[1][0] = lds_rd4(ard1 + 16384);
  Af[1][1] = lds_rd4(ard1 + 18432);
  Af[1][2] = lds_rd4(ard1 + 20480);
  Af[1][3] = lds_rd4(ard1 + 22528);
  Ar = lds_rd4(ars + 16384);
  SB();
  MF(acc[0][0], "v", Bf[6][0], "v", Af[0][0]);
  MF(acc[0][1], "v", Bf[6][1], "v", Af[0][0]);
  MF(acc[0][2], "v", Bf[6][2], "v", Af[0][0]);
  MF(acc[0][3], "v", Bf[6][3], "v", Af[0][0]);
  SB();
  LD4V_SC1(mg[2], tid16, sws, WS_MODG + 8192);
  SB();
  MF(acc[1][0], "v", Bf[6][0], "v", Af[0][1]);
  MF(acc[1][1], "v", Bf[6][1], "v", Af[0][1]);
  MF(acc[1][2], "v", Bf[6][2], "v", Af[0][1]);
  MF(acc[1][3], "v", Bf[6][3], "v", Af[0][1]);
  SB();
  LD4A_SC1I(wd[9], l16, sws, wdb + 1 * WD_NT + 0, 2048);
  SB();
  MF(acc[2][0], "v", Bf[6][0], "v", Af[0][2]);
  MF(acc[2][1], "v", Bf[6][1], "v", Af[0][2]);
  MF(acc[2][2], "v", Bf[6][2], "v", Af[0][2]);
  MF(acc[2][3], "v", Bf[6][3], "v", Af[0][2]);
  SB();
  LD4A_SC1I(wd[13], l16, sws, wdb + 1 * WD_NT + 0, 3072);
  LD4A_SC1I(wd[10], l16, sws, wdb + 2 * WD_NT + 0, 2048);
  SB();
  MF(acc[3][0], "v", Bf[6][0], "v", Af[0][3]);
  MF(acc[3][1], "v", Bf[6][1], "v", Af[0][3]);
  MF(acc[3][2], "v", Bf[6][2], "v", Af[0][3]);
  MF(acc[3][3], "v", Bf[6][3], "v", Af[0][3]);
  MF(rs[0], "v", ones, "v", Ar);
  SB();
  LD4A_SC1I(wd[14], l16, sws, wdb + 2 * WD_NT + 0, 3072);
  SB();
  WAITV(33);
  PINV(Bf[6][4]);
  PINV(Bf[6][5]);
  PINV(Bf[6][6]);
  PINV(Bf[6][7]);
  Af[0][0] = lds_rd4(ard0 + 24576);
  Af[0][1] = lds_rd4(ard0 + 26624);
  Af[0][2] = lds_rd4(ard0 + 28672);
  Af[0][3] = lds_rd4(ard0 + 30720);
  SB();
  MF(acc[0][0], "v", Bf[6][4], "v", Af[1][0]);
  MF(acc[0][1], "v", Bf[6][5], "v", Af[1][0]);
  MF(acc[0][2], "v", Bf[6][6], "v", Af[1][0]);
  MF(acc[0][3], "v", Bf[6][7], "v", Af[1][0]);
  SB();
  LD4A_SC1I(wd[11], l16, sws, wdb + 3 * WD_NT + 0, 2048);
  SB();
  MF(acc[1][0], "v", Bf[6][4], "v", Af[1][1]);
  MF(acc[1][1], "v", Bf[6][5], "v", Af[1][1]);
  MF(acc[1][2], "v", Bf[6][6], "v", Af[1][1]);
  MF(acc[1][3], "v", Bf[6][7], "v", Af[1][1]);
  SB();
  LD4A_SC1I(wd[15], l16, sws, wdb + 3 * WD_NT + 0, 3072);
  SB();
  MF(acc[2][0], "v", Bf[6][4], "v", Af[1][2]);
  MF(acc[2][1], "v", Bf[6][5], "v", Af[1][2]);
  MF(acc[2][2], "v", Bf[6][6], "v", Af[1][2]);
  MF(acc[2][3], "v", Bf[6][7], "v", Af[1][2]);
  SB();
  LD4A_SC1I(wd[16], l16, sws, wdb + 0 * WD_NT + 16384, 0);
  LD4A_SC1I(wd[20], l16, sws, wdb + 0 * WD_NT + 16384, 1024);
  SB();
  MF(acc[3][0], "v", Bf[6][4], "v", Af[1][3]);
  MF(acc[3][1], "v", Bf[6][5], "v", Af[1][3]);
  MF(acc[3][2], "v", Bf[6][6], "v", Af[1][3]);
  MF(acc[3][3], "v", Bf[6][7], "v", Af[1][3]);
  SB();
  LD4A_SC1I(wd[17], l16, sws, wdb + 1 * WD_NT + 16384, 0);
  SB();
  WAITV(29);
  PINV(Bf[7][0]);
  PINV(Bf[7][1]);
  PINV(Bf[7][2]);
  PINV(Bf[7][3]);
  Af[1][0] = lds_rd4(ard1 + 24576);
  Af[1][1] = lds_rd4(ard1 + 26624);
  Af[1][2] = lds_rd4(ard1 + 28672);
  Af[1][3] = lds_rd4(ard1 + 30720);
  Ar = lds_rd4(ars + 24576);
  SB();
  MF(acc[0][0], "v", Bf[7][0], "v", Af[0][0]);
  MF(acc[0][1], "v", Bf[7][1], "v", Af[0][0]);
  MF(acc[0][2], "v", Bf[7][2], "v", Af[0][0]);
  MF(acc[0][3], "v", Bf[7][3], "v", Af[0][0]);
  SB();
  LD4A_SC1I(wd[21], l16, sws, wdb + 1 * WD_NT + 16384, 1024);
  SB();
  MF(acc[1][0], "v", Bf[7][0], "v", Af[0][1]);
  MF(acc[1][1], "v", Bf[7][1], "v", Af[0][1]);
  MF(acc[1][2], "v", Bf[7][2], "v", Af[0][1]);
  MF(acc[1][3], "v", Bf[7][3], "v", Af[0][1]);
  SB();
  LD4A_SC1I(wd[18], l16, sws, wdb + 2 * WD_NT + 16384, 0);
  WAITV(11);
  PINV(mg[0]);
  PINV(mg[1]);
  PINV(mg[2]);
  MOD_STAGE();
  SB();
  MF(acc[2][0], "v", Bf[7][0], "v", Af[0][2]);
  MF(acc[2][1], "v", Bf[7][1], "v", Af[0][2]);
  MF(acc[2][2], "v", Bf[7][2], "v", Af[0][2]);
  MF(acc[2][3], "v", Bf[7][3], "v", Af[0][2]);
  SB();
  LD4A_SC1I(wd[22], l16, sws, wdb + 2 * WD_NT + 16384, 1024);
  LD4A_SC1I(wd[19], l16, sws, wdb + 3 * WD_NT + 16384, 0);
  SB();
  MF(acc[3][0], "v", Bf[7][0], "v", Af[0][3]);
  MF(acc[3][1], "v", Bf[7][1], "v", Af[0][3]);
  MF(acc[3][2], "v", Bf[7][2], "v", Af[0][3]);
  MF(acc[3][3], "v", Bf[7][3], "v", Af[0][3]);
  MF(rs[0], "v", ones, "v", Ar);
  SB();
  LD4A_SC1I(wd[23], l16, sws, wdb + 3 * WD_NT + 16384, 1024);
  SB();
  PINV(Bf[7][4]);
  PINV(Bf[7][5]);
  PINV(Bf[7][6]);
  PINV(Bf[7][7]);
  BAR();
  Af[0][0] = lds_rd4(ard0 + 32768);
  Af[0][1] = lds_rd4(ard0 + 34816);
  Af[0][2] = lds_rd4(ard0 + 36864);
  Af[0][3] = lds_rd4(ard0 + 38912);
  SB();
  MF(acc[2][0], "v", Bf[7][4], "v", Af[1][2]);
  MF(acc[2][1], "v", Bf[7][5], "v", Af[1][2]);
  MF(acc[2][2], "v", Bf[7][6], "v", Af[1][2]);
  MF(acc[2][3], "v", Bf[7][7], "v", Af[1][2]);
  SB();
  LD4A_SC1I(wd[24], l16, sws, wdb + 0 * WD_NT + 16384, 2048);
  SB();
  MF(acc[3][0], "v", Bf[7][4], "v", Af[1][3]);
  MF(acc[3][1], "v", Bf[7][5], "v", Af[1][3]);
  MF(acc[3][2], "v", Bf[7][6], "v", Af[1][3]);
  MF(acc[3][3], "v", Bf[7][7], "v", Af[1][3]);
  SB();
  LD4A_SC1I(wd[28], l16, sws, wdb + 0 * WD_NT + 16384, 3072);
  asm volatile("s_nop 1" : "+a"(acc[2][0]), "+a"(acc[2][1]), "+a"(acc[2][2]), "+a"(acc[2][3]));
  lds_wr4f(xw + 0, acc[2][0]);
  lds_wr4f(xw + 1024, acc[2][1]);
  lds_wr4f(xw + 2048, acc[2][2]);
  lds_wr4f(xw + 3072, acc[2][3]);
  SB();
  MF(acc[0][0], "v", Bf[7][4], "v", Af[1][0]);
  MF(acc[0][1], "v", Bf[7][5], "v", Af[1][0]);
  MF(acc[0][2], "v", Bf[7][6], "v", Af[1][0]);
  MF(acc[0][3], "v", Bf[7][7], "v", Af[1][0]);
  SB();
  LD4A_SC1I(wd[25], l16, sws, wdb + 1 * WD_NT + 16384, 2048);
  LD4A_SC1I(wd[29], l16, sws, wdb + 1 * WD_NT + 16384, 3072);
  asm volatile("" : "+a"(acc[3][0]), "+a"(acc[3][1]), "+a"(acc[3][2]), "+a"(acc[3][3]));
  lds_wr4f(xw + 4096, acc[3][0]);
  lds_wr4f(xw + 5120, acc[3][1]);
  lds_wr4f(xw + 6144, acc[3][2]);
  lds_wr4f(xw + 7168, acc[3][3]);
  SB();
  MF(acc[1][0], "v", Bf[7][4], "v", Af[1][1]);
  MF(acc[1][1], "v", Bf[7][5], "v", Af[1][1]);
  MF(acc[1][2], "v", Bf[7][6], "v", Af[1][1]);
  MF(acc[1][3], "v", Bf[7][7], "v", Af[1][1]);
  SB();
  LD4V_SC1I(wd[26], l16, sws, wdb + 2 * WD_NT + 16384, 2048);
  SB();
  BAR();
  Af[1][0] = lds_rd4(ard1 + 32768);
  Af[1][1] = lds_rd4(ard1 + 34816);
  Af[1][2] = lds_rd4(ard1 + 36864);
  Af[1][3] = lds_rd4(ard1 + 38912);
  Ar = lds_rd4(ars + 32768);
  pr[0][0] = lds_rd4f(xr + 0);
  pr[0][1] = lds_rd4f(xr + 1024);
  pr[0][2] = lds_rd4f(xr + 2048);
  pr[0][3] = lds_rd4f(xr + 3072);
  pr[1][0] = lds_rd4f(xr + 4096);
  pr[1][1] = lds_rd4f(xr + 5120);
  pr[1][2] = lds_rd4f(xr + 6144);
  pr[1][3] = lds_rd4f(xr + 7168);
  asm volatile("s_nop 6" : "+a"(acc[0][0]), "+a"(acc[0][1]), "+a"(acc[0][2]), "+a"(acc[0][3]), "+a"(acc[1][0]), "+a"(acc[1][1]), "+a"(acc[1][2]), "+a"(acc[1][3]));
  SB();
  MF(acc[4][0], "v", Bf[4][0], "v", Af[0][0]);
  MF(acc[4][1], "v", Bf[4][1], "v", Af[0][0]);
  MF(acc[4][2], "v", Bf[4][2], "v", Af[0][0]);
  MF(acc[4][3], "v", Bf[4][3], "v", Af[0][0]);
  SB();
  LD4V_SC1I(wd[30], l16, sws, wdb + 2 * WD_NT + 16384, 3072);
  SB();
  MF(acc[5][0], "v", Bf[4][0], "v", Af[0][1]);
  SB();
  PQ1(ux, 0, 0);
  SB();
  MF(acc[5][1], "v", Bf[4][1], "v", Af[0][1]);
  SB();
  PQ1(uy, 0, 1);
  SB();
  MF(acc[5][2], "v", Bf[4][2], "v", Af[0][1]);
  SB();
  pq = v4u{ux.x, ux.y, uy.x, uy.y};
  ST4_SC1(pq, l16, sws, xco + 0);
  SB();
  MF(acc[5][3], "v", Bf[4][3], "v", Af[0][1]);
  SB();
  PQ1(ux, 0, 2);
  SB();
  SB();
  LD4V_SC1I(wd[27], l16, sws, wdb + 3 * WD_NT + 16384, 2048);
  SB();
  MF(acc[6][0], "v", Bf[4][0], "v", Af[0][2]);
  SB();
  PQ1(uy, 0, 3);
  SB();
  MF(acc[6][1], "v", Bf[4][1], "v", Af[0][2]);
  SB();
  pq = v4u{ux.x, ux.y, uy.x, uy.y};
  ST4_SC1(pq, l16, sws, xco + 1024);
  SB();
  MF(acc[6][2], "v", Bf[4][2], "v", Af[0][2]);
  SB();
  PQ1(ux, 1, 0);
  SB();
  MF(acc[6][3], "v", Bf[4][3], "v", Af[0][2]);
  SB();
  PQ1(uy, 1, 1);
  SB();
  SB();
  LD4V_SC1I(wd[31], l16, sws, wdb + 3 * WD_NT + 16384, 3072);
  LD2V(xe[0], xev, sx, xes + 0);
  SB();
  MF(acc[7][0], "v", Bf[4][0], "v", Af[0][3]);
  SB();
  pq = v4u{ux.x, ux.y, uy.x, uy.y};
  ST4_SC1(pq, l16, sws, xco + 2048);
  SB();
  MF(acc[7][1], "v", Bf[4][1], "v", Af[0][3]);
  SB();
  PQ1(ux, 1, 2);
  SB();
  MF(acc[7][2], "v", Bf[4][2], "v", Af[0][3]);
  SB();
  PQ1(uy, 1, 3);
  SB();
  MF(acc[7][3], "v", Bf[4][3], "v", Af[0][3]);
  SB();
  pq = v4u{ux.x, ux.y, uy.x, uy.y};
  ST4_SC1(pq, l16, sws, xco + 3072);
  SB();
  MF(rs[1], "v", ones, "v", Ar);
  SB();
  LD2V(xe[1], xev, sx, xes + 32);
  SB();
  Af[0][0] = lds_rd4(ard0 + 40960);
  Af[0][1] = lds_rd4(ard0 + 43008);
  Af[0][2] = lds_rd4(ard0 + 45056);
  Af[0][3] = lds_rd4(ard0 + 47104);
  SB();
  MF(acc[4][0], "v", Bf[4][4], "v", Af[1][0]);
  MF(acc[4][1], "v", Bf[4][5], "v", Af[1][0]);
  MF(acc[4][2], "v", Bf[4][6], "v", Af[1][0]);
  MF(acc[4][3], "v", Bf[4][7], "v", Af[1][0]);
  SB();
  LD2V(xe[2], xev, sx, xes + 64);
  SB();
  MF(acc[5][0], "v", Bf[4][4], "v", Af[1][1]);
  MF(acc[5][1], "v", Bf[4][5], "v", Af[1][1]);
  MF(acc[5][2], "v", Bf[4][6], "v", Af[1][1]);
  MF(acc[5][3], "v", Bf[4][7], "v", Af[1][1]);
  SB();
  LD2V(xe[3], xev, sx, xes + 96);
  SB();
  MF(acc[6][0], "v", Bf[4][4], "v", Af[1][2]);
  MF(acc[6][1], "v", Bf[4][5], "v", Af[1][2]);
  MF(acc[6][2], "v", Bf[4][6], "v", Af[1][2]);
  MF(acc[6][3], "v", Bf[4][7], "v", Af[1][2]);
  SB();
  LD4A_SC1I(w1f[0], l16, sws, w1b0 + 0, 0);
  LD4A_SC1I(w1f[3], l16, sws, w1b0 + 0, 1024);
  SB();
  MF(acc[7][0], "v", Bf[4][4], "v", Af[1][3]);
  MF(acc[7][1], "v", Bf[4][5], "v", Af[1][3]);
  MF(acc[7][2], "v", Bf[4][6], "v", Af[1][3]);
  MF(acc[7][3], "v", Bf[4][7], "v", Af[1][3]);
  SB();
  SB();
  Af[1][0] = lds_rd4(ard1 + 40960);
  Af[1][1] = lds_rd4(ard1 + 43008);
  Af[1][2] = lds_rd4(ard1 + 45056);
  Af[1][3] = lds_rd4(ard1 + 47104);
  Ar = lds_rd4(ars + 40960);
  SB();
  MF(acc[4][0], "v", Bf[5][0], "v", Af[0][0]);
  MF(acc[4][1], "v", Bf[5][1], "v", Af[0][0]);
  MF(acc[4][2], "v", Bf[5][2], "v", Af[0][0]);
  MF(acc[4][3], "v", Bf[5][3], "v", Af[0][0]);
  SB();
  SB();
  MF(acc[5][0], "v", Bf[5][0], "v", Af[0][1]);
  MF(acc[5][1], "v", Bf[5][1], "v", Af[0][1]);
  MF(acc[5][2], "v", Bf[5][2], "v", Af[0][1]);
  MF(acc[5][3], "v", Bf[5][3], "v", Af[0][1]);
  SB();
  SB();
  MF(acc[6][0], "v", Bf[5][0], "v", Af[0][2]);
  MF(acc[6][1], "v", Bf[5][1], "v", Af[0][2]);
  MF(acc[6][2], "v", Bf[5][2], "v", Af[0][2]);
  MF(acc[6][3], "v", Bf[5][3], "v", Af[0][2]);
  SB();
  SB();
  MF(acc[7][0], "v", Bf[5][0], "v", Af[0][3]);
  MF(acc[7][1], "v", Bf[5][1], "v", Af[0][3]);
  MF(acc[7][2], "v", Bf[5][2], "v", Af[0][3]);
  MF(acc[7][3], "v", Bf[5][3], "v", Af[0][3]);
  MF(rs[1], "v", ones, "v", Ar);
  SB();
  SB();
  Af[0][0] = lds_rd4(ard0 + 49152);
  Af[0][1] = lds_rd4(ard0 + 51200);
  Af[0][2] = lds_rd4(ard0 + 53248);
  Af[0][3] = lds_rd4(ard0 + 55296);
  SB();
  MF(acc[4][0], "v", Bf[5][4], "v", Af[1][0]);
  MF(acc[4][1], "v", Bf[5][5], "v", Af[1][0]);
  MF(acc[4][2], "v", Bf[5][6], "v", Af[1][0]);
  MF(acc[4][3], "v", Bf[5][7], "v", Af[1][0]);
  SB();
  SB();
  MF(acc[5][0], "v", Bf[5][4], "v", Af[1][1]);
  MF(acc[5][1], "v", Bf[5][5], "v", Af[1][1]);
  MF(acc[5][2], "v", Bf[5][6], "v", Af[1][1]);
  MF(acc[5][3], "v", Bf[5][7], "v", Af[1][1]);
  SB();
  SB();
  MF(acc[6][0], "v", Bf[5][4], "v", Af[1][2]);
  MF(acc[6][1], "v", Bf[5][5], "v", Af[1][2]);
  MF(acc[6][2], "v", Bf[5][6], "v", Af[1][2]);
  MF(acc[6][3], "v", Bf[5][7], "v", Af[1][2]);
  SB();
  WAITV(5);
  ST1_SC1(tag, lane0, sws, xfo);
  SB();
  MF(acc[7][0], "v", Bf[5][4], "v", Af[1][3]);
  MF(acc[7][1], "v", Bf[5][5], "v", Af[1][3]);
  MF(acc[7][2], "v", Bf[5][6], "v", Af[1][3]);
  MF(acc[7][3], "v", Bf[5][7], "v", Af[1][3]);
  SB();
  SB();
  Af[1][0] = lds_rd4(ard1 + 49152);
  Af[1][1] = lds_rd4(ard1 + 51200);
  Af[1][2] = lds_rd4(ard1 + 53248);
  Af[1][3] = lds_rd4(ard1 + 55296);
  Ar = lds_rd4(ars + 49152);
  SB();
  MF(acc[4][0], "v", Bf[6][0], "v", Af[0][0]);
  MF(acc[4][1], "v", Bf[6][1], "v", Af[0][0]);
  MF(acc[4][2], "v", Bf[6][2], "v", Af[0][0]);
  MF(acc[4][3], "v", Bf[6][3], "v", Af[0][0]);
  SB();
  SB();
  MF(acc[5][0], "v", Bf[6][0], "v", Af[0][1]);
  MF(acc[5][1], "v", Bf[6][1], "v", Af[0][1]);
  MF(acc[5][2], "v", Bf[6][2], "v", Af[0][1]);
  MF(acc[5][3], "v", Bf[6][3], "v", Af[0][1]);
  SB();
  SB();
  MF(acc[6][0], "v", Bf[6][0], "v", Af[0][2]);
  MF(acc[6][1], "v", Bf[6][1], "v", Af[0][2]);
  MF(acc[6][2], "v", Bf[6][2], "v", Af[0][2]);
  MF(acc[6][3], "v", Bf[6][3], "v", Af[0][2]);
  SB();
  SB();
  MF(acc[7][0], "v", Bf[6][0], "v", Af[0][3]);
  MF(acc[7][1], "v", Bf[6][1], "v", Af[0][3]);
  MF(acc[7][2], "v", Bf[6][2], "v", Af[0][3]);
  MF(acc[7][3], "v", Bf[6][3], "v", Af[0][3]);
  MF(rs[1], "v", ones, "v", Ar);
  SB();
  SB();
  Af[0][0] = lds_rd4(ard0 + 57344);
  Af[0][1] = lds_rd4(ard0 + 59392);
  Af[0][2] = lds_rd4(ard0 + 61440);
  Af[0][3] = lds_rd4(ard0 + 63488);
  SB();
  MF(acc[4][0], "v", Bf[6][4], "v", Af[1][0]);
  MF(acc[4][1], "v", Bf[6][5], "v", Af[1][0]);
  MF(acc[4][2], "v", Bf[6][6], "v", Af[1][0]);
  MF(acc[4][3], "v", Bf[6][7], "v", Af[1][0]);
  SB();
  SB();
  MF(acc[5][0], "v", Bf[6][4], "v", Af[1][1]);
  MF(acc[5][1], "v", Bf[6][5], "v", Af[1][1]);
  MF(acc[5][2], "v", Bf[6][6], "v", Af[1][1]);
  MF(acc[5][3], "v", Bf[6][7], "v", Af[1][1]);
  SB();
  SB();
  MF(acc[6][0], "v", Bf[6][4], "v", Af[1][2]);
  MF(acc[6][1], "v", Bf[6][5], "v", Af[1][2]);
  MF(acc[6][2], "v", Bf[6][6], "v", Af[1][2]);
  MF(acc[6][3], "v", Bf[6][7], "v", Af[1][2]);
  SB();
  LD1V_SC1(f1, zero, sws, xfi);
  SB();
  MF(acc[7][0], "v", Bf[6][4], "v", Af[1][3]);
  MF(acc[7][1], "v", Bf[6][5], "v", Af[1][3]);
  MF(acc[7][2], "v", Bf[6][6], "v", Af[1][3]);
  MF(acc[7][3], "v", Bf[6][7], "v", Af[1][3]);
  SB();
  SB();
  Af[1][0] = lds_rd4(ard1 + 57344);
  Af[1][1] = lds_rd4(ard1 + 59392);
  Af[1][2] = lds_rd4(ard1 + 61440);
  Af[1][3] = lds_rd4(ard1 + 63488);
  Ar = lds_rd4(ars + 57344);
  SB();
  MF(acc[4][0], "v", Bf[7][0], "v", Af[0][0]);
  MF(acc[4][1], "v", Bf[7][1], "v", Af[0][0]);
  MF(acc[4][2], "v", Bf[7][2], "v", Af[0][0]);
  MF(acc[4][3], "v", Bf[7][3], "v", Af[0][0]);
  SB();
  gr[0] = lds_rd2(mcolA + 0);
  gr[1] = lds_rd2(mcolA + 32);
  gr[2] = lds_rd2(mcolA + 64);
  gr[3] = lds_rd2(mcolA + 96);
  sf[0] = lds_rd4(sfb + 0);
  sf[1] = lds_rd4(sfb + 64);
  sf[2] = lds_rd4(sfb + 128);
  sf[3] = lds_rd4(sfb + 192);
  SB();
  MF(acc[5][0], "v", Bf[7][0], "v", Af[0][1]);
  MF(acc[5][1], "v", Bf[7][1], "v", Af[0][1]);
  MF(acc[5][2], "v", Bf[7][2], "v", Af[0][1]);
  MF(acc[5][3], "v", Bf[7][3], "v", Af[0][1]);
  SB();
  SB();
  MF(acc[6][0], "v", Bf[7][0], "v", Af[0][2]);
  MF(acc[6][1], "v", Bf[7][1], "v", Af[0][2]);
  MF(acc[6][2], "v", Bf[7][2], "v", Af[0][2]);
  MF(acc[6][3], "v", Bf[7][3], "v", Af[0][2]);
  SB();
  SB();
  MF(acc[7][0], "v", Bf[7][0], "v", Af[0][3]);
  SB();
  UNPK(gv[0], gr[0]); gv[0] *= sgn; PINV(gv[0]);
  SB();
  MF(acc[7][1], "v", Bf[7][1], "v", Af[0][3]);
  SB();
  UNPK(gv[1], gr[1]); gv[1] *= sgn; PINV(gv[1]);
  SB();
  MF(acc[7][2], "v", Bf[7][2], "v", Af[0][3]);
  SB();
  UNPK(gv[2], gr[2]); gv[2] *= sgn; PINV(gv[2]);
  SB();
  MF(acc[7][3], "v", Bf[7][3], "v", Af[0][3]);
  SB();
  UNPK(gv[3], gr[3]); gv[3] *= sgn; PINV(gv[3]);
  SB();
  MF(rs[1], "v", ones, "v", Ar);
  SB();
  WAITV(0);
  PINV(f1);
  SPIN(!__all(f1 == tag), POLL1(f1, zero, xfi));
  LD4V_SC1I(xp[0], l16, sws, xci, 0);
  LD4V_SC1I(xp[1], l16, sws, xci, 1024);
  LD4V_SC1I(xp[2], l16, sws, xci, 2048);
  LD4V_SC1I(xp[3], l16, sws, xci, 3072);
  SB();
  SB();
  MF(acc[6][0], "v", Bf[7][4], "v", Af[1][2]);
  MF(acc[6][1], "v", Bf[7][5], "v", Af[1][2]);
  MF(acc[6][2], "v", Bf[7][6], "v", Af[1][2]);
  MF(acc[6][3], "v", Bf[7][7], "v", Af[1][2]);
  SB();
  asm volatile("" : "+a"(rs[0]), "+a"(rs[1]));
  lds_wr1f(rsw, rs[0][0]); lds_wr1f(rsw + 64, rs[1][0]);
  SB();
  MF(acc[7][0], "v", Bf[7][4], "v", Af[1][3]);
  MF(acc[7][1], "v", Bf[7][5], "v", Af[1][3]);
  MF(acc[7][2], "v", Bf[7][6], "v", Af[1][3]);
  MF(acc[7][3], "v", Bf[7][7], "v", Af[1][3]);
  SB();
  asm volatile("s_nop 3" : "+a"(acc[6][0]), "+a"(acc[6][1]), "+a"(acc[6][2]), "+a"(acc[6][3]));
  lds_wr4f(xw + 16384, acc[6][0]);
  lds_wr4f(xw + 17408, acc[6][1]);
  lds_wr4f(xw + 18432, acc[6][2]);
  lds_wr4f(xw + 19456, acc[6][3]);
  SB();
  MF(acc[4][0], "v", Bf[7][4], "v", Af[1][0]);
  MF(acc[4][1], "v", Bf[7][5], "v", Af[1][0]);
  MF(acc[4][2], "v", Bf[7][6], "v", Af[1][0]);
  MF(acc[4][3], "v", Bf[7][7], "v", Af[1][0]);
  SB();
  asm volatile("" : "+a"(acc[7][0]), "+a"(acc[7][1]), "+a"(acc[7][2]), "+a"(acc[7][3]));
  lds_wr4f(xw + 20480, acc[7][0]);
  lds_wr4f(xw + 21504, acc[7][1]);
  lds_wr4f(xw + 22528, acc[7][2]);
  lds_wr4f(xw + 23552, acc[7][3]);
  SB();
  MF(acc[5][0], "v", Bf[7][4], "v", Af[1][1]);
  MF(acc[5][1], "v", Bf[7][5], "v", Af[1][1]);
  MF(acc[5][2], "v", Bf[7][6], "v", Af[1][1]);
  MF(acc[5][3], "v", Bf[7][7], "v", Af[1][1]);
  SB();
  SB();
  PINV(cpf);
  BAR();
  pr[0][0] = lds_rd4f(xr + 16384);
  pr[0][1] = lds_rd4f(xr + 17408);
  pr[0][2] = lds_rd4f(xr + 18432);
  pr[0][3] = lds_rd4f(xr + 19456);
  pr[1][0] = lds_rd4f(xr + 20480);
  pr[1][1] = lds_rd4f(xr + 21504);
  pr[1][2] = lds_rd4f(xr + 22528);
  pr[1][3] = lds_rd4f(xr + 23552);
  float rx[8];
  rx[0] = lds_rd1f(lds0 + L_RSX + 0 + r * 4);
  rx[1] = lds_rd1f(lds0 + L_RSX + 64 + r * 4);
  rx[2] = lds_rd1f(lds0 + L_RSX + 128 + r * 4);
  rx[3] = lds_rd1f(lds0 + L_RSX + 192 + r * 4);
  rx[4] = lds_rd1f(lds0 + L_RSX + 256 + r * 4);
  rx[5] = lds_rd1f(lds0 + L_RSX + 320 + r * 4);
  rx[6] = lds_rd1f(lds0 + L_RSX + 384 + r * 4);
  rx[7] = lds_rd1f(lds0 + L_RSX + 448 + r * 4);
  SB();
  asm volatile("s_nop 7" : "+a"(acc[4][0]), "+a"(acc[4][1]), "+a"(acc[4][2]), "+a"(acc[4][3]), "+a"(acc[5][0]), "+a"(acc[5][1]), "+a"(acc[5][2]), "+a"(acc[5][3]));
  WAITV(3);
  PINV(xp[0]);
  { v2u X, Y; PQ_TILE(X, 4, 0, xp[0].x, xp[0].y); PQ_TILE(Y, 4, 1, xp[0].z, xp[0].w); pq = swap_pack(X, Y); }
  ST4_SC1(pq, vfr, sws, pqs + 0 * PQ_KS + 0);
  WAITV(3);
  PINV(xp[1]);
  { v2u X, Y; PQ_TILE(X, 4, 2, xp[1].x, xp[1].y); PQ_TILE(Y, 4, 3, xp[1].z, xp[1].w); pq = swap_pack(X, Y); }
  ST4_SC1(pq, vfr, sws, pqs + 1 * PQ_KS + 0);
  WAITV(3);
  PINV(xp[2]);
  { v2u X, Y; PQ_TILE(X, 5, 0, xp[2].x, xp[2].y); PQ_TILE(Y, 5, 1, xp[2].z, xp[2].w); pq = swap_pack(X, Y); }
  ST4_SC1(pq, vfr, sws, pqs + 0 * PQ_KS + 1024);
  WAITV(3);
  PINV(xp[3]);
  { v2u X, Y; PQ_TILE(X, 5, 2, xp[3].x, xp[3].y); PQ_TILE(Y, 5, 3, xp[3].z, xp[3].w); pq = swap_pack(X, Y); }
  ST4_SC1(pq, vfr, sws, pqs + 1 * PQ_KS + 1024);
  { const float re0 = (rx[0] + rx[2]) + (rx[4] + rx[6]), rl1 = (rx[1] + rx[3]) + (rx[5] + rx[7]); pq = rs_piece(kh ? re0 : rl1, kh ? rl1 : re0); }
  ST4_SC1(pq, g4 == 0 ? r * 16 : OOB, sws, w == 0 ? WS_RS + tb * RS_TB + cj * 512 + kh * 256 : OOB);
  LD4A_SC1I(w1f[6], l16, sws, w1b0 + 0, 2048);
  LD4A_SC1I(w1f[9], l16, sws, w1b0 + 0, 3072);
  LD4A_SC1I(w1f[1], l16, sws, w1b1 + 0, 0);
  LD4A_SC1I(w1f[4], l16, sws, w1b1 + 0, 1024);
  LD4A_SC1I(w1f[7], l16, sws, w1b1 + 0, 2048);
  LD4A_SC1I(w1f[10], l16, sws, w1b1 + 0, 3072);
  LD4A_SC1I(w1f[2], l16, sws, w1b2 + 0, 0);
  LD4A_SC1I(w1f[5], l16, sws, w1b2 + 0, 1024);
  LD4V_SC1I(w1f[8], l16, sws, w1b2 + 0, 2048);
  LD4V_SC1I(w1f[11], l16, sws, w1b2 + 0, 3072);
  PINA(wd[0]);
  PINA(wd[1]);
  PINA(wd[2]);
  PINA(wd[3]);
  PINA(wd[4]);
  PINA(wd[5]);
  PINA(wd[6]);
  PINA(wd[7]);
  PINA(wd[8]);
  PINA(wd[9]);
  PINA(wd[10]);
  PINA(wd[11]);
  PINA(wd[12]);
  PINA(wd[13]);
  PINA(wd[14]);
  PINA(wd[15]);
  PINA(wd[16]);
  PINA(wd[17]);
  PINA(wd[18]);
  PINA(wd[19]);
  PINA(wd[20]);
  PINA(wd[21]);
  PINA(wd[22]);
  PINA(wd[23]);
  PINA(wd[24]);
  PINA(wd[25]);
  PINV(wd[26]);
  PINV(wd[27]);
  PINA(wd[28]);
  PINA(wd[29]);
  PINV(wd[30]);
  PINV(wd[31]);
  MF0(zz[0][0], "v", sf[0], "a", wd[0]);
  MF0(zz[0][1], "v", sf[0], "a", wd[1]);
  MF0(zz[0][2], "v", sf[0], "a", wd[2]);
  MF0(zz[0][3], "v", sf[0], "a", wd[3]);
  MF(zz[0][0], "v", sf[1], "a", wd[4]);
  MF(zz[0][1], "v", sf[1], "a", wd[5]);
  MF(zz[0][2], "v", sf[1], "a", wd[6]);
  MF(zz[0][3], "v", sf[1], "a", wd[7]);
  MF(zz[0][0], "v", sf[2], "a", wd[8]);
  MF(zz[0][1], "v", sf[2], "a", wd[9]);
  MF(zz[0][2], "v", sf[2], "a", wd[10]);
  MF(zz[0][3], "v", sf[2], "a", wd[11]);
  MF(zz[0][0], "v", sf[3], "a", wd[12]);
  MF(zz[0][1], "v", sf[3], "a", wd[13]);
  MF(zz[0][2], "v", sf[3], "a", wd[14]);
  MF(zz[0][3], "v", sf[3], "a", wd[15]);
  MF0(zz[1][0], "v", sf[0], "a", wd[16]);
  MF0(zz[1][1], "v", sf[0], "a", wd[17]);
  MF0(zz[1][2], "v", sf[0], "a", wd[18]);
  MF0(zz[1][3], "v", sf[0], "a", wd[19]);
  MF(zz[1][0], "v", sf[1], "a", wd[20]);
  MF(zz[1][1], "v", sf[1], "a", wd[21]);
  MF(zz[1][2], "v", sf[1], "a", wd[22]);
  MF(zz[1][3], "v", sf[1], "a", wd[23]);
  MF(zz[1][0], "v", sf[2], "a", wd[24]);
  MF(zz[1][1], "v", sf[2], "a", wd[25]);
  MF(zz[1][2], "v", sf[2], "v", wd[26]);
  MF(zz[1][3], "v", sf[2], "v", wd[27]);
  MF(zz[1][0], "v", sf[3], "a", wd[28]);
  MF(zz[1][1], "v", sf[3], "a", wd[29]);
  MF(zz[1][2], "v", sf[3], "v", wd[30]);
  MF(zz[1][3], "v", sf[3], "v", wd[31]);
  SB();
  WAITV(10);
  BAR();
  ST1_SC1(tag, lane0, sws, flo + 1024);
  LD4V_SC1I(w1f[12], l16, sws, w1b0 + 4096, 0);
  LD4V_SC1I(w1f[15], l16, sws, w1b0 + 4096, 1024);
  LD4V_SC1I(w1f[18], l16, sws, w1b0 + 4096, 2048);
  LD4V_SC1I(w1f[21], l16, sws, w1b0 + 4096, 3072);
  LD4V_SC1I(w1f[13], l16, sws, w1b1 + 4096, 0);
  LD4V_SC1I(w1f[16], l16, sws, w1b1 + 4096, 1024);
  LD4V_SC1I(w1f[19], l16, sws, w1b1 + 4096, 2048);
  LD4V_SC1I(w1f[22], l16, sws, w1b1 + 4096, 3072);
  LD4V_SC1I(w1f[14], l16, sws, w1b2 + 4096, 0);
  LD4V_SC1I(w1f[17], l16, sws, w1b2 + 4096, 1024);
  LD4V_SC1I(w1f[20], l16, sws, w1b2 + 4096, 2048);
  LD4V_SC1I(w1f[23], l16, sws, w1b2 + 4096, 3072);
  asm volatile("" : "+a"(zz[0][0]), "+a"(zz[1][0]));
  zf[0] = z_piece(zz[0][0][0], zz[1][0][0], g4); PINV(zf[0]);
  asm volatile("" : "+a"(zz[0][1]), "+a"(zz[1][1]));
  zf[1] = z_piece(zz[0][1][0], zz[1][1][0], g4); PINV(zf[1]);
  asm volatile("" : "+a"(zz[0][2]), "+a"(zz[1][2]));
  zf[2] = z_piece(zz[0][2][0], zz[1][2][0], g4); PINV(zf[2]);
  asm volatile("" : "+a"(zz[0][3]), "+a"(zz[1][3]));
  zf[3] = z_piece(zz[0][3][0], zz[1][3][0], g4); PINV(zf[3]);
  LD1V_SC1(f1, fv8, sws, flg + 1024);
  gr[0] = lds_rd2(mcol + 2048); gr2[0] = lds_rd2(mcol + 4096);
  gr[1] = lds_rd2(mcol + 2080); gr2[1] = lds_rd2(mcol + 4128);
  gr[2] = lds_rd2(mcol + 2112); gr2[2] = lds_rd2(mcol + 4160);
  gr[3] = lds_rd2(mcol + 2144); gr2[3] = lds_rd2(mcol + 4192);
  UNPK(gv[0], gr[0]); UNPK(g2[0], gr2[0]);
  UNPK(gv[1], gr[1]); UNPK(g2[1], gr2[1]);
  UNPK(gv[2], gr[2]); UNPK(g2[2], gr2[2]);
  UNPK(gv[3], gr[3]); UNPK(g2[3], gr2[3]);
  PINV(xe[0]);
  PINV(xe[1]);
  PINV(xe[2]);
  PINV(xe[3]);
  WAITV(0);
  PINV(f1);
  SPIN(!__all(f1 == tag), POLL1(f1, fv8, flg + 1024));
  LD4V_SC1(pa[0], vrot[0], sws, pab + 0 * PQ_KS);
  LD4V_SC1(pa[1], vrot[1], sws, pab + 0 * PQ_KS);
  LD4V_SC1(pa[2], vrot[2], sws, pab + 0 * PQ_KS);
  LD4V_SC1(pa[3], vrot[3], sws, pab + 0 * PQ_KS);
  LD4V_SC1(pa[4], vrot[0], sws, pab + 1 * PQ_KS);
  LD4V_SC1(pa[5], vrot[1], sws, pab + 1 * PQ_KS);
  LD4V_SC1(pa[6], vrot[2], sws, pab + 1 * PQ_KS);
  LD4V_SC1(pa[7], vrot[3], sws, pab + 1 * PQ_KS);
  LD4V_SC1(pa[8], vrot[0], sws, pab + 2 * PQ_KS);
  LD4V_SC1(pa[9], vrot[1], sws, pab + 2 * PQ_KS);
  LD4V_SC1(pa[10], vrot[2], sws, pab + 2 * PQ_KS);
  LD4V_SC1(pa[11], vrot[3], sws, pab + 2 * PQ_KS);
  LD4V_SC1(pa[12], vrot[0], sws, pab + 3 * PQ_KS);
  LD4V_SC1(pa[13], vrot[1], sws, pab + 3 * PQ_KS);
  LD4V_SC1(pa[14], vrot[2], sws, pab + 3 * PQ_KS);
  LD4V_SC1(pa[15], vrot[3], sws, pab + 3 * PQ_KS);
  WAITV(12);
  PINV(pa[0]);
  PINV(pa[1]);
  PINV(pa[2]);
  PINV(pa[3]);
  MF0(a1[0][0], "a", wd[0], "v", pa[0]);
  MF0(a1[0][1], "a", wd[1], "v", pa[0]);
  MF0(a1[0][2], "a", wd[2], "v", pa[0]);
  MF0(a1[0][3], "a", wd[3], "v", pa[0]);
  SB();
  LD4V_SC1(pa[16], vrot[0], sws, pab + 16 * PQ_KS);
  SB();
  MF0(a1[1][0], "a", wd[0], "v", pa[1]);
  MF0(a1[1][1], "a", wd[1], "v", pa[1]);
  MF0(a1[1][2], "a", wd[2], "v", pa[1]);
  MF0(a1[1][3], "a", wd[3], "v", pa[1]);
  SB();
  LD4V_SC1(pa[17], vrot[1], sws, pab + 16 * PQ_KS);
  SB();
  MF0(a1[2][0], "a", wd[0], "v", pa[2]);
  MF0(a1[2][1], "a", wd[1], "v", pa[2]);
  MF0(a1[2][2], "a", wd[2], "v", pa[2]);
  MF0(a1[2][3], "a", wd[3], "v", pa[2]);
  SB();
  LD4V_SC1(pa[18], vrot[2], sws, pab + 16 * PQ_KS);
  SB();
  MF0(a1[3][0], "a", wd[0], "v", pa[3]);
  MF0(a1[3][1], "a", wd[1], "v", pa[3]);
  MF0(a1[3][2], "a", wd[2], "v", pa[3]);
  MF0(a1[3][3], "a", wd[3], "v", pa[3]);
  SB();
  LD4V_SC1(pa[19], vrot[3], sws, pab + 16 * PQ_KS);
  SB();
  WAITV(12);
  PINV(pa[4]);
  PINV(pa[5]);
  PINV(pa[6]);
  PINV(pa[7]);
  MF(a1[0][0], "a", wd[4], "v", pa[4]);
  MF(a1[0][1], "a", wd[5], "v", pa[4]);
  MF(a1[0][2], "a", wd[6], "v", pa[4]);
  MF(a1[0][3], "a", wd[7], "v", pa[4]);
  SB();
  LD4V_SC1(pa[20], vrot[0], sws, pab + 17 * PQ_KS);
  SB();
  MF(a1[1][0], "a", wd[4], "v", pa[5]);
  MF(a1[1][1], "a", wd[5], "v", pa[5]);
  MF(a1[1][2], "a", wd[6], "v", pa[5]);
  MF(a1[1][3], "a", wd[7], "v", pa[5]);
  SB();
  LD4V_SC1(pa[21], vrot[1], sws, pab + 17 * PQ_KS);
  SB();
  MF(a1[2][0], "a", wd[4], "v", pa[6]);
  MF(a1[2][1], "a", wd[5], "v", pa[6]);
  MF(a1[2][2], "a", wd[6], "v", pa[6]);
  MF(a1[2][3], "a", wd[7], "v", pa[6]);
  SB();
  LD4V_SC1(pa[22], vrot[2], sws, pab + 17 * PQ_KS);
  SB();
  MF(a1[3][0], "a", wd[4], "v", pa[7]);
  MF(a1[3][1], "a", wd[5], "v", pa[7]);
  MF(a1[3][2], "a", wd[6], "v", pa[7]);
  MF(a1[3][3], "a", wd[7], "v", pa[7]);
  SB();
  LD4V_SC1(pa[23], vrot[3], sws, pab + 17 * PQ_KS);
  SB();
  WAITV(12);
  PINV(pa[8]);
  PINV(pa[9]);
  PINV(pa[10]);
  PINV(pa[11]);
  MF(a1[0][0], "a", wd[8], "v", pa[8]);
  MF(a1[0][1], "a", wd[9], "v", pa[8]);
  MF(a1[0][2], "a", wd[10], "v", pa[8]);
  MF(a1[0][3], "a", wd[11], "v", pa[8]);
  SB();
  LD4V_SC1(rf[0], vrs[0], sws, WS_RS + tb * RS_TB);
  SB();
  MF(a1[1][0], "a", wd[8], "v", pa[9]);
  MF(a1[1][1], "a", wd[9], "v", pa[9]);
  MF(a1[1][2], "a", wd[10], "v", pa[9]);
  MF(a1[1][3], "a", wd[11], "v", pa[9]);
  SB();
  LD4V_SC1(rf[1], vrs[1], sws, WS_RS + tb * RS_TB);
  SB();
  MF(a1[2][0], "a", wd[8], "v", pa[10]);
  MF(a1[2][1], "a", wd[9], "v", pa[10]);
  MF(a1[2][2], "a", wd[10], "v", pa[10]);
  MF(a1[2][3], "a", wd[11], "v", pa[10]);
  SB();
  LD4V_SC1(rf[2], vrs[2], sws, WS_RS + tb * RS_TB);
  SB();
  MF(a1[3][0], "a", wd[8], "v", pa[11]);
  MF(a1[3][1], "a", wd[9], "v", pa[11]);
  MF(a1[3][2], "a", wd[10], "v", pa[11]);
  MF(a1[3][3], "a", wd[11], "v", pa[11]);
  SB();
  LD4V_SC1(rf[3], vrs[3], sws, WS_RS + tb * RS_TB);
  SB();
  WAITV(12);
  PINV(pa[12]);
  PINV(pa[13]);
  PINV(pa[14]);
  PINV(pa[15]);
  MF(a1[0][0], "a", wd[12], "v", pa[12]);
  MF(a1[0][1], "a", wd[13], "v", pa[12]);
  MF(a1[0][2], "a", wd[14], "v", pa[12]);
  MF(a1[0][3], "a", wd[15], "v", pa[12]);
  SB();
  LD4V_SC1(pa[24], vrot[0], sws, pab + 18 * PQ_KS);
  SB();
  MF(a1[1][0], "a", wd[12], "v", pa[13]);
  MF(a1[1][1], "a", wd[13], "v", pa[13]);
  MF(a1[1][2], "a", wd[14], "v", pa[13]);
  MF(a1[1][3], "a", wd[15], "v", pa[13]);
  SB();
  LD4V_SC1(pa[25], vrot[1], sws, pab + 18 * PQ_KS);
  SB();
  MF(a1[2][0], "a", wd[12], "v", pa[14]);
  MF(a1[2][1], "a", wd[13], "v", pa[14]);
  MF(a1[2][2], "a", wd[14], "v", pa[14]);
  MF(a1[2][3], "a", wd[15], "v", pa[14]);
  SB();
  LD4V_SC1(pa[26], vrot[2], sws, pab + 18 * PQ_KS);
  SB();
  MF(a1[3][0], "a", wd[12], "v", pa[15]);
  MF(a1[3][1], "a", wd[13], "v", pa[15]);
  MF(a1[3][2], "a", wd[14], "v", pa[15]);
  MF(a1[3][3], "a", wd[15], "v", pa[15]);
  SB();
  LD4V_SC1(pa[27], vrot[3], sws, pab + 18 * PQ_KS);
  SB();
  WAITV(12);
  PINV(pa[16]);
  PINV(pa[17]);
  PINV(pa[18]);
  PINV(pa[19]);
  MF(a1[0][0], "a", wd[16], "v", pa[16]);
  MF(a1[0][1], "a", wd[17], "v", pa[16]);
  MF(a1[0][2], "a", wd[18], "v", pa[16]);
  MF(a1[0][3], "a", wd[19], "v", pa[16]);
  SB();
  LD4V_SC1(pa[28], vrot[0], sws, pab + 19 * PQ_KS);
  SB();
  MF(a1[1][0], "a", wd[16], "v", pa[17]);
  MF(a1[1][1], "a", wd[17], "v", pa[17]);
  MF(a1[1][2], "a", wd[18], "v", pa[17]);
  MF(a1[1][3], "a", wd[19], "v", pa[17]);
  SB();
  LD4V_SC1(pa[29], vrot[1], sws, pab + 19 * PQ_KS);
  SB();
  MF(a1[2][0], "a", wd[16], "v", pa[18]);
  MF(a1[2][1], "a", wd[17], "v", pa[18]);
  MF(a1[2][2], "a", wd[18], "v", pa[18]);
  MF(a1[2][3], "a", wd[19], "v", pa[18]);
  SB();
  LD4V_SC1(pa[30], vrot[2], sws, pab + 19 * PQ_KS);
  SB();
  MF(a1[3][0], "a", wd[16], "v", pa[19]);
  MF(a1[3][1], "a", wd[17], "v", pa[19]);
  MF(a1[3][2], "a", wd[18], "v", pa[19]);
  MF(a1[3][3], "a", wd[19], "v", pa[19]);
  SB();
  LD4V_SC1(pa[31], vrot[3], sws, pab + 19 * PQ_KS);
  SB();
  LD4V(cpf, l16, scode, (w == 3 && idx >= 19) ? idx * 1024 : OOB);
  WAITV(13);
  PINV(pa[20]);
  PINV(pa[21]);
  PINV(pa[22]);
  PINV(pa[23]);
  MF(a1[0][0], "a", wd[20], "v", pa[20]);
  MF(a1[0][1], "a", wd[21], "v", pa[20]);
  MF(a1[0][2], "a", wd[22], "v", pa[20]);
  MF(a1[0][3], "a", wd[23], "v", pa[20]);
  SB();
  LD4V_SC1I(w1f[24], l16, sws, w1b0 + 8192, 0);
  SB();
  MF(a1[1][0], "a", wd[20], "v", pa[21]);
  MF(a1[1][1], "a", wd[21], "v", pa[21]);
  MF(a1[1][2], "a", wd[22], "v", pa[21]);
  MF(a1[1][3], "a", wd[23], "v", pa[21]);
  SB();
  LD4V_SC1I(w1f[27], l16, sws, w1b0 + 8192, 1024);
  SB();
  MF(a1[2][0], "a", wd[20], "v", pa[22]);
  MF(a1[2][1], "a", wd[21], "v", pa[22]);
  MF(a1[2][2], "a", wd[22], "v", pa[22]);
  MF(a1[2][3], "a", wd[23], "v", pa[22]);
  SB();
  LD4V_SC1I(w1f[30], l16, sws, w1b0 + 8192, 2048);
  SB();
  MF(a1[3][0], "a", wd[20], "v", pa[23]);
  MF(a1[3][1], "a", wd[21], "v", pa[23]);
  MF(a1[3][2], "a", wd[22], "v", pa[23]);
  MF(a1[3][3], "a", wd[23], "v", pa[23]);
  SB();
  LD4V_SC1I(w1f[33], l16, sws, w1b0 + 8192, 3072);
  SB();
  WAITV(13);
  PINV(rf[0]);
  PINV(rf[1]);
  PINV(rf[2]);
  PINV(rf[3]);
  MF(a1[0][0], "v", zf[0], "v", rf[0]);
  MF(a1[0][1], "v", zf[1], "v", rf[0]);
  MF(a1[0][2], "v", zf[2], "v", rf[0]);
  MF(a1[0][3], "v", zf[3], "v", rf[0]);
  SB();
  LD4V_SC1I(w1f[25], l16, sws, w1b1 + 8192, 0);
  SB();
  MF(a1[1][0], "v", zf[0], "v", rf[1]);
  MF(a1[1][1], "v", zf[1], "v", rf[1]);
  MF(a1[1][2], "v", zf[2], "v", rf[1]);
  MF(a1[1][3], "v", zf[3], "v", rf[1]);
  SB();
  LD4V_SC1I(w1f[28], l16, sws, w1b1 + 8192, 1024);
  SB();
  MF(a1[2][0], "v", zf[0], "v", rf[2]);
  MF(a1[2][1], "v", zf[1], "v", rf[2]);
  MF(a1[2][2], "v", zf[2], "v", rf[2]);
  MF(a1[2][3], "v", zf[3], "v", rf[2]);
  SB();
  LD4V_SC1I(w1f[31], l16, sws, w1b1 + 8192, 2048);
  SB();
  MF(a1[3][0], "v", zf[0], "v", rf[3]);
  MF(a1[3][1], "v", zf[1], "v", rf[3]);
  MF(a1[3][2], "v", zf[2], "v", rf[3]);
  MF(a1[3][3], "v", zf[3], "v", rf[3]);
  SB();
  LD4V_SC1I(w1f[34], l16, sws, w1b1 + 8192, 3072);
  SB();
  WAITV(13);
  PINV(pa[24]);
  PINV(pa[25]);
  PINV(pa[26]);
  PINV(pa[27]);
  MF(a1[0][0], "a", wd[24], "v", pa[24]);
  MF(a1[0][1], "a", wd[25], "v", pa[24]);
  MF(a1[0][2], "v", wd[26], "v", pa[24]);
  MF(a1[0][3], "v", wd[27], "v", pa[24]);
  SB();
  LD4V_SC1I(w1f[26], l16, sws, w1b2 + 8192, 0);
  SB();
  MF(a1[1][0], "a", wd[24], "v", pa[25]);
  MF(a1[1][1], "a", wd[25], "v", pa[25]);
  MF(a1[1][2], "v", wd[26], "v", pa[25]);
  MF(a1[1][3], "v", wd[27], "v", pa[25]);
  SB();
  LD4V_SC1I(w1f[29], l16, sws, w1b2 + 8192, 1024);
  SB();
  MF(a1[2][0], "a", wd[24], "v", pa[26]);
  MF(a1[2][1], "a", wd[25], "v", pa[26]);
  MF(a1[2][2], "v", wd[26], "v", pa[26]);
  MF(a1[2][3], "v", wd[27], "v", pa[26]);
  SB();
  LD4A_SC1I(w1f[32], l16, sws, w1b2 + 8192, 2048);
  SB();
  MF(a1[3][0], "a", wd[24], "v", pa[27]);
  MF(a1[3][1], "a", wd[25], "v", pa[27]);
  MF(a1[3][2], "v", wd[26], "v", pa[27]);
  MF(a1[3][3], "v", wd[27], "v", pa[27]);
  SB();
  LD4A_SC1I(w1f[35], l16, sws, w1b2 + 8192, 3072);
  SB();
  WAITV(13);
  PINV(pa[28]);
  PINV(pa[29]);
  PINV(pa[30]);
  PINV(pa[31]);
  MF(a1[1][0], "a", wd[28], "v", pa[29]);
  MF(a1[1][1], "a", wd[29], "v", pa[29]);
  MF(a1[1][2], "v", wd[30], "v", pa[29]);
  MF(a1[1][3], "v", wd[31], "v", pa[29]);
  SB();
  SB();
  MF(a1[2][0], "a", wd[28], "v", pa[30]);
  MF(a1[2][1], "a", wd[29], "v", pa[30]);
  MF(a1[2][2], "v", wd[30], "v", pa[30]);
  MF(a1[2][3], "v", wd[31], "v", pa[30]);
  SB();
  asm volatile("s_nop 3" : "+a"(a1[1][0]), "+a"(a1[1][1]), "+a"(a1[1][2]), "+a"(a1[1][3]));
  lds_wr4f(red + 0, a1[1][0]);
  lds_wr4f(red + 1024, a1[1][1]);
  lds_wr4f(red + 2048, a1[1][2]);
  lds_wr4f(red + 3072, a1[1][3]);
  SB();
  MF(a1[3][0], "a", wd[28], "v", pa[31]);
  MF(a1[3][1], "a", wd[29], "v", pa[31]);
  MF(a1[3][2], "v", wd[30], "v", pa[31]);
  MF(a1[3][3], "v", wd[31], "v", pa[31]);
  SB();
  asm volatile("" : "+a"(a1[2][0]), "+a"(a1[2][1]), "+a"(a1[2][2]), "+a"(a1[2][3]));
  lds_wr4f(red + 4096, a1[2][0]);
  lds_wr4f(red + 5120, a1[2][1]);
  lds_wr4f(red + 6144, a1[2][2]);
  lds_wr4f(red + 7168, a1[2][3]);
  SB();
  MF(a1[0][0], "a", wd[28], "v", pa[28]);
  MF(a1[0][1], "a", wd[29], "v", pa[28]);
  MF(a1[0][2], "v", wd[30], "v", pa[28]);
  MF(a1[0][3], "v", wd[31], "v", pa[28]);
  SB();
  asm volatile("s_nop 3" : "+a"(a1[3][0]), "+a"(a1[3][1]), "+a"(a1[3][2]), "+a"(a1[3][3]));
  lds_wr4f(red + 8192, a1[3][0]);
  lds_wr4f(red + 9216, a1[3][1]);
  lds_wr4f(red + 10240, a1[3][2]);
  lds_wr4f(red + 11264, a1[3][3]);
  SB();
  WAITV(12);
  PINV(cpf);
  BAR();
  pr[0][0] = lds_rd4f(rdb1 + 0);
  pr[0][1] = lds_rd4f(rdb1 + 1024);
  pr[0][2] = lds_rd4f(rdb1 + 2048);
  pr[0][3] = lds_rd4f(rdb1 + 3072);
  pr[1][0] = lds_rd4f(rdb2 + 4096);
  pr[1][1] = lds_rd4f(rdb2 + 5120);
  pr[1][2] = lds_rd4f(rdb2 + 6144);
  pr[1][3] = lds_rd4f(rdb2 + 7168);
  pr[2][0] = lds_rd4f(rdb3 + 8192);
  pr[2][1] = lds_rd4f(rdb3 + 9216);
  pr[2][2] = lds_rd4f(rdb3 + 10240);
  pr[2][3] = lds_rd4f(rdb3 + 11264);
  SB();
  asm volatile("s_nop 3" : "+a"(a1[0][0]), "+a"(a1[0][1]), "+a"(a1[0][2]), "+a"(a1[0][3]));
  G1_EPI();
  pq = swap_pack(xs[0], xs[1]);
  ST4_SC1(pq, vfr, sws, WS_X1F + tb * X1_TB + ((2 * j + 0) * 4 + w) * 1024);
  pq = swap_pack(xs[2], xs[3]);
  ST4_SC1(pq, vfr, sws, WS_X1F + tb * X1_TB + ((2 * j + 1) * 4 + w) * 1024);
  G1_STATS();
  ST2_SC1(st2, g4 == 0 ? r * 16 : OOB, sws, WS_STAT + tb * ST_TB + (j >> 1) * 1024 + w * 256 + (j & 1) * 8);
  LD4A_SC1I(w1f[36], l16, sws, w1b0 + 12288, 0);
  LD4A_SC1I(w1f[39], l16, sws, w1b0 + 12288, 1024);
  LD4A_SC1I(w1f[42], l16, sws, w1b0 + 12288, 2048);
  LD4A_SC1I(w1f[45], l16, sws, w1b0 + 12288, 3072);
  LD4A_SC1I(w1f[37], l16, sws, w1b1 + 12288, 0);
  LD4A_SC1I(w1f[40], l16, sws, w1b1 + 12288, 1024);
  LD4A_SC1I(w1f[43], l16, sws, w1b1 + 12288, 2048);
  LD4A_SC1I(w1f[46], l16, sws, w1b1 + 12288, 3072);
  gs[0] = lds_rd4(gsb + 0);
  gs[1] = lds_rd4(gsb + 64);
  gs[2] = lds_rd4(gsb + 128);
  gs[3] = lds_rd4(gsb + 192);
  gs[4] = lds_rd4(gsb + 256);
  gs[5] = lds_rd4(gsb + 320);
  gs[6] = lds_rd4(gsb + 384);
  gs[7] = lds_rd4(gsb + 448);
  gs[8] = lds_rd4(gsb + 512);
  gs[9] = lds_rd4(gsb + 576);
  gs[10] = lds_rd4(gsb + 640);
  gs[11] = lds_rd4(gsb + 704);
  gs[12] = lds_rd4(gsb + 768);
  gs[13] = lds_rd4(gsb + 832);
  gs[14] = lds_rd4(gsb + 896);
  gs[15] = lds_rd4(gsb + 960);
  WAITV(8);
  BAR();
  ST1_SC1(tag, lane0, sws, flo + 2048);
  LD4A_SC1I(w1f[38], l16, sws, w1b2 + 12288, 0);
  LD4A_SC1I(w1f[41], l16, sws, w1b2 + 12288, 1024);
  LD4A_SC1I(w1f[44], l16, sws, w1b2 + 12288, 2048);
  LD4A_SC1I(w1f[47], l16, sws, w1b2 + 12288, 3072);
  PINA(w1f[0]);
  PINA(w1f[1]);
  PINA(w1f[2]);
  MF0(cc[0], "a", w1f[0], "v", gs[0]);
  MF0(cc[1], "a", w1f[1], "v", gs[0]);
  MF0(cc[2], "a", w1f[2], "v", gs[0]);
  SB();
  LD4A_SC1I(w2f[0], l16, sws, w2b + 0 * W2_NT + 0, 0);
  LD4A_SC1I(w2f[4], l16, sws, w2b + 0 * W2_NT + 0, 1024);
  SB();
  PINA(w1f[3]);
  PINA(w1f[4]);
  PINA(w1f[5]);
  MF(cc[0], "a", w1f[3], "v", gs[1]);
  MF(cc[1], "a", w1f[4], "v", gs[1]);
  MF(cc[2], "a", w1f[5], "v", gs[1]);
  SB();
  LD4A_SC1I(w2f[8], l16, sws, w2b + 0 * W2_NT + 0, 2048);
  LD4A_SC1I(w2f[12], l16, sws, w2b + 0 * W2_NT + 0, 3072);
  SB();
  PINA(w1f[6]);
  PINA(w1f[7]);
  PINV(w1f[8]);
  MF(cc[0], "a", w1f[6], "v", gs[2]);
  MF(cc[1], "a", w1f[7], "v", gs[2]);
  MF(cc[2], "v", w1f[8], "v", gs[2]);
  SB();
  LD4A_SC1I(w2f[16], l16, sws, w2b + 0 * W2_NT + 4096, 0);
  LD4A_SC1I(w2f[1], l16, sws, w2b + 1 * W2_NT + 0, 0);
  SB();
  PINA(w1f[9]);
  PINA(w1f[10]);
  PINV(w1f[11]);
  MF(cc[0], "a", w1f[9], "v", gs[3]);
  MF(cc[1], "a", w1f[10], "v", gs[3]);
  MF(cc[2], "v", w1f[11], "v", gs[3]);
  SB();
  LD4A_SC1I(w2f[5], l16, sws, w2b + 1 * W2_NT + 0, 1024);
  LD4A_SC1I(w2f[9], l16, sws, w2b + 1 * W2_NT + 0, 2048);
  SB();
  PINV(w1f[12]);
  PINV(w1f[13]);
  PINV(w1f[14]);
  MF(cc[0], "v", w1f[12], "v", gs[4]);
  MF(cc[1], "v", w1f[13], "v", gs[4]);
  MF(cc[2], "v", w1f[14], "v", gs[4]);
  SB();
  LD4A_SC1I(w2f[13], l16, sws, w2b + 1 * W2_NT + 0, 3072);
  LD4A_SC1I(w2f[17], l16, sws, w2b + 1 * W2_NT + 4096, 0);
  SB();
  PINV(w1f[15]);
  PINV(w1f[16]);
  PINV(w1f[17]);
  MF(cc[0], "v", w1f[15], "v", gs[5]);
  MF(cc[1], "v", w1f[16], "v", gs[5]);
  MF(cc[2], "v", w1f[17], "v", gs[5]);
  SB();
  SB();
  PINV(w1f[18]);
  PINV(w1f[19]);
  PINV(w1f[20]);
  MF(cc[0], "v", w1f[18], "v", gs[6]);
  MF(cc[1], "v", w1f[19], "v", gs[6]);
  MF(cc[2], "v", w1f[20], "v", gs[6]);
  SB();
  SB();
  PINV(w1f[21]);
  PINV(w1f[22]);
  PINV(w1f[23]);
  MF(cc[0], "v", w1f[21], "v", gs[7]);
  MF(cc[1], "v", w1f[22], "v", gs[7]);
  MF(cc[2], "v", w1f[23], "v", gs[7]);
  SB();
  SB();
  PINV(w1f[24]);
  PINV(w1f[25]);
  PINV(w1f[26]);
  MF(cc[0], "v", w1f[24], "v", gs[8]);
  MF(cc[1], "v", w1f[25], "v", gs[8]);
  MF(cc[2], "v", w1f[26], "v", gs[8]);
  SB();
  SB();
  PINV(w1f[27]);
  PINV(w1f[28]);
  PINV(w1f[29]);
  MF(cc[0], "v", w1f[27], "v", gs[9]);
  MF(cc[1], "v", w1f[28], "v", gs[9]);
  MF(cc[2], "v", w1f[29], "v", gs[9]);
  SB();
  SB();
  PINV(w1f[30]);
  PINV(w1f[31]);
  PINA(w1f[32]);
  MF(cc[0], "v", w1f[30], "v", gs[10]);
  MF(cc[1], "v", w1f[31], "v", gs[10]);
  MF(cc[2], "a", w1f[32], "v", gs[10]);
  SB();
  SB();
  PINV(w1f[33]);
  PINV(w1f[34]);
  PINA(w1f[35]);
  MF(cc[0], "v", w1f[33], "v", gs[11]);
  MF(cc[1], "v", w1f[34], "v", gs[11]);
  MF(cc[2], "a", w1f[35], "v", gs[11]);
  SB();
  LD1V_SC1(f1, fv2, sws, flg + 2048);
  SB();
  WAITV(14);
  PINA(w1f[36]);
  PINA(w1f[37]);
  PINA(w1f[38]);
  MF(cc[0], "a", w1f[36], "v", gs[12]);
  MF(cc[1], "a", w1f[37], "v", gs[12]);
  MF(cc[2], "a", w1f[38], "v", gs[12]);
  SB();
  SB();
  WAITV(13);
  PINA(w1f[39]);
  PINA(w1f[40]);
  PINA(w1f[41]);
  MF(cc[0], "a", w1f[39], "v", gs[13]);
  MF(cc[1], "a", w1f[40], "v", gs[13]);
  MF(cc[2], "a", w1f[41], "v", gs[13]);
  SB();
  SB();
  WAITV(12);
  PINA(w1f[42]);
  PINA(w1f[43]);
  PINA(w1f[44]);
  MF(cc[0], "a", w1f[42], "v", gs[14]);
  MF(cc[1], "a", w1f[43], "v", gs[14]);
  MF(cc[2], "a", w1f[44], "v", gs[14]);
  SB();
  SB();
  WAITV(11);
  PINA(w1f[45]);
  PINA(w1f[46]);
  PINA(w1f[47]);
  MF(cc[0], "a", w1f[45], "v", gs[15]);
  MF(cc[1], "a", w1f[46], "v", gs[15]);
  MF(cc[2], "a", w1f[47], "v", gs[15]);
  SB();
  SB();
  asm volatile("s_nop 7" : "+a"(cc[0]), "+a"(cc[1]), "+a"(cc[2]));
  C12();
  WAITV(0);
  PINV(f1);
  SPIN(!__all(f1 == tag), POLL1(f1, fv2, flg + 2048));
  DMAI_SC1(a2w, 0, l16, sws, x1s + 0);
  DMAI_SC1(a2w, 1024, l16, sws, x1s + 1024);
  DMAI_SC1(a2w, 4096, l16, sws, x1s + 4096);
  DMAI_SC1(a2w, 5120, l16, sws, x1s + 5120);
  DMAI_SC1(a2w, 8192, l16, sws, x1s + 8192);
  DMAI_SC1(a2w, 9216, l16, sws, x1s + 9216);
  DMAI_SC1(a2w, 12288, l16, sws, x1s + 12288);
  DMAI_SC1(a2w, 13312, l16, sws, x1s + 13312);
  DMAI_SC1(a2w, 2048, l16, sws, x1s + 2048);
  DMAI_SC1(a2w, 3072, l16, sws, x1s + 3072);
  DMAI_SC1(a2w, 6144, l16, sws, x1s + 6144);
  DMAI_SC1(a2w, 7168, l16, sws, x1s + 7168);
  DMAI_SC1(a2w, 10240, l16, sws, x1s + 10240);
  DMAI_SC1(a2w, 11264, l16, sws, x1s + 11264);
  DMAI_SC1(a2w, 14336, l16, sws, x1s + 14336);
  DMAI_SC1(a2w, 15360, l16, sws, x1s + 15360);
  WAITV(8);
  BAR();
  LD4V_SC1I(stt[0], l16, sws, WS_STAT + tb * ST_TB, 0);
  LD4V_SC1I(stt[1], l16, sws, WS_STAT + tb * ST_TB, 1024);
  LD4V_SC1I(stt[2], l16, sws, WS_STAT + tb * ST_TB, 2048);
  LD4V_SC1I(stt[3], l16, sws, WS_STAT + tb * ST_TB, 3072);
  PINV(a2r);
  A2[0][0] = lds_rd4(a2r + 0);
  A2[0][1] = lds_rd4(a2r + 1024);
  A2[1][0] = lds_rd4(a2r + 4096);
  A2[1][1] = lds_rd4(a2r + 5120);
  A2[2][0] = lds_rd4(a2r + 8192);
  A2[2][1] = lds_rd4(a2r + 9216);
  SB();
  MF0(a2[0][0], "a", w1f[0], "v", A2[0][0]);
  MF0(a2[0][1], "a", w1f[1], "v", A2[0][0]);
  MF0(a2[0][2], "a", w1f[2], "v", A2[0][0]);
  SB();
  SB();
  MF0(a2[1][0], "a", w1f[0], "v", A2[0][1]);
  MF0(a2[1][1], "a", w1f[1], "v", A2[0][1]);
  MF0(a2[1][2], "a", w1f[2], "v", A2[0][1]);
  SB();
  SB();
  A2[0][0] = lds_rd4(a2r + 12288);
  A2[0][1] = lds_rd4(a2r + 13312);
  SB();
  MF(a2[0][0], "a", w1f[3], "v", A2[1][0]);
  MF(a2[0][1], "a", w1f[4], "v", A2[1][0]);
  MF(a2[0][2], "a", w1f[5], "v", A2[1][0]);
  SB();
  SB();
  MF(a2[1][0], "a", w1f[3], "v", A2[1][1]);
  MF(a2[1][1], "a", w1f[4], "v", A2[1][1]);
  MF(a2[1][2], "a", w1f[5], "v", A2[1][1]);
  SB();
  SB();
  A2[1][0] = lds_rd4(a2r + 16384);
  A2[1][1] = lds_rd4(a2r + 17408);
  SB();
  MF(a2[0][0], "a", w1f[6], "v", A2[2][0]);
  MF(a2[0][1], "a", w1f[7], "v", A2[2][0]);
  MF(a2[0][2], "v", w1f[8], "v", A2[2][0]);
  SB();
  SB();
  MF(a2[1][0], "a", w1f[6], "v", A2[2][1]);
  MF(a2[1][1], "a", w1f[7], "v", A2[2][1]);
  MF(a2[1][2], "v", w1f[8], "v", A2[2][1]);
  SB();
  SB();
  A2[2][0] = lds_rd4(a2r + 20480);
  A2[2][1] = lds_rd4(a2r + 21504);
  SB();
  MF(a2[0][0], "a", w1f[9], "v", A2[0][0]);
  MF(a2[0][1], "a", w1f[10], "v", A2[0][0]);
  MF(a2[0][2], "v", w1f[11], "v", A2[0][0]);
  SB();
  LD4A_SC1I(w2f[2], l16, sws, w2b + 2 * W2_NT + 0, 0);
  SB();
  MF(a2[1][0], "a", w1f[9], "v", A2[0][1]);
  MF(a2[1][1], "a", w1f[10], "v", A2[0][1]);
  MF(a2[1][2], "v", w1f[11], "v", A2[0][1]);
  SB();
  LD4A_SC1I(w2f[6], l16, sws, w2b + 2 * W2_NT + 0, 1024);
  SB();
  A2[0][0] = lds_rd4(a2r + 24576);
  A2[0][1] = lds_rd4(a2r + 25600);
  SB();
  MF(a2[0][0], "v", w1f[12], "v", A2[1][0]);
  MF(a2[0][1], "v", w1f[13], "v", A2[1][0]);
  MF(a2[0][2], "v", w1f[14], "v", A2[1][0]);
  SB();
  LD4A_SC1I(w2f[10], l16, sws, w2b + 2 * W2_NT + 0, 2048);
  SB();
  MF(a2[1][0], "v", w1f[12], "v", A2[1][1]);
  MF(a2[1][1], "v", w1f[13], "v", A2[1][1]);
  MF(a2[1][2], "v", w1f[14], "v", A2[1][1]);
  SB();
  SB();
  A2[1][0] = lds_rd4(a2r + 28672);
  A2[1][1] = lds_rd4(a2r + 29696);
  SB();
  MF(a2[0][0], "v", w1f[15], "v", A2[2][0]);
  MF(a2[0][1], "v", w1f[16], "v", A2[2][0]);
  MF(a2[0][2], "v", w1f[17], "v", A2[2][0]);
  SB();
  LD4A_SC1I(w2f[14], l16, sws, w2b + 2 * W2_NT + 0, 3072);
  SB();
  MF(a2[1][0], "v", w1f[15], "v", A2[2][1]);
  MF(a2[1][1], "v", w1f[16], "v", A2[2][1]);
  MF(a2[1][2], "v", w1f[17], "v", A2[2][1]);
  SB();
  LD4A_SC1I(w2f[18], l16, sws, w2b + 2 * W2_NT + 4096, 0);
  SB();
  A2[2][0] = lds_rd4(a2r + 32768);
  A2[2][1] = lds_rd4(a2r + 33792);
  SB();
  MF(a2[0][0], "v", w1f[18], "v", A2[0][0]);
  MF(a2[0][1], "v", w1f[19], "v", A2[0][0]);
  MF(a2[0][2], "v", w1f[20], "v", A2[0][0]);
  SB();
  LD4A_SC1I(w2f[3], l16, sws, w2b + 3 * W2_NT + 0, 0);
  SB();
  MF(a2[1][0], "v", w1f[18], "v", A2[0][1]);
  MF(a2[1][1], "v", w1f[19], "v", A2[0][1]);
  MF(a2[1][2], "v", w1f[20], "v", A2[0][1]);
  SB();
  SB();
  A2[0][0] = lds_rd4(a2r + 36864);
  A2[0][1] = lds_rd4(a2r + 37888);
  SB();
  MF(a2[0][0], "v", w1f[21], "v", A2[1][0]);
  MF(a2[0][1], "v", w1f[22], "v", A2[1][0]);
  MF(a2[0][2], "v", w1f[23], "v", A2[1][0]);
  SB();
  LD4A_SC1I(w2f[7], l16, sws, w2b + 3 * W2_NT + 0, 1024);
  SB();
  MF(a2[1][0], "v", w1f[21], "v", A2[1][1]);
  MF(a2[1][1], "v", w1f[22], "v", A2[1][1]);
  MF(a2[1][2], "v", w1f[23], "v", A2[1][1]);
  SB();
  LD4A_SC1I(w2f[11], l16, sws, w2b + 3 * W2_NT + 0, 2048);
  SB();
  WAITV(12);
  BARN();
  A2[1][0] = lds_rd4(a2r + 40960);
  A2[1][1] = lds_rd4(a2r + 41984);
  SB();
  MF(a2[0][0], "v", w1f[24], "v", A2[2][0]);
  MF(a2[0][1], "v", w1f[25], "v", A2[2][0]);
  MF(a2[0][2], "v", w1f[26], "v", A2[2][0]);
  SB();
  LD4A_SC1I(w2f[15], l16, sws, w2b + 3 * W2_NT + 0, 3072);
  SB();
  MF(a2[1][0], "v", w1f[24], "v", A2[2][1]);
  MF(a2[1][1], "v", w1f[25], "v", A2[2][1]);
  MF(a2[1][2], "v", w1f[26], "v", A2[2][1]);
  SB();
  SB();
  A2[2][0] = lds_rd4(a2r + 45056);
  A2[2][1] = lds_rd4(a2r + 46080);
  SB();
  WAITV(9);
  PINV(stt[0]);
  PINV(stt[1]);
  PINV(stt[2]);
  PINV(stt[3]);
  MF(a2[0][0], "v", w1f[27], "v", A2[0][0]);
  ROWSTAT(0);
  SB();
  MF(a2[0][1], "v", w1f[28], "v", A2[0][0]);
  ROWSTAT(1);
  SB();
  MF(a2[0][2], "v", w1f[29], "v", A2[0][0]);
  ROWSTAT(2);
  SB();
  SB();
  LD4A_SC1I(w2f[19], l16, sws, w2b + 3 * W2_NT + 4096, 0);
  SB();
  MF(a2[1][0], "v", w1f[27], "v", A2[0][1]);
  ROWSTAT(3);
  SB();
  MF(a2[1][1], "v", w1f[28], "v", A2[0][1]);
  ROWSTAT(4);
  SB();
  MF(a2[1][2], "v", w1f[29], "v", A2[0][1]);
  ROWSTAT(5);
  SB();
  SB();
  LD4A_SC1I(w2f[20], l16, sws, w2b + 0 * W2_NT + 5120, 0);
  SB();
  A2[0][0] = lds_rd4(a2r + 49152);
  A2[0][1] = lds_rd4(a2r + 50176);
  SB();
  MF(a2[0][0], "v", w1f[30], "v", A2[1][0]);
  ROWSTAT(6);
  SB();
  MF(a2[0][1], "v", w1f[31], "v", A2[1][0]);
  ROWSTAT(7);
  SB();
  MF(a2[0][2], "a", w1f[32], "v", A2[1][0]);
  ROWSTAT(8);
  SB();
  SB();
  LD4A_SC1I(w2f[24], l16, sws, w2b + 0 * W2_NT + 5120, 1024);
  SB();
  MF(a2[1][0], "v", w1f[30], "v", A2[1][1]);
  ROWSTAT(9);
  SB();
  MF(a2[1][1], "v", w1f[31], "v", A2[1][1]);
  ROWSTAT(10);
  SB();
  MF(a2[1][2], "a", w1f[32], "v", A2[1][1]);
  ROWSTAT(11);
  SB();
  SB();
  SB();
  A2[1][0] = lds_rd4(a2r + 53248);
  A2[1][1] = lds_rd4(a2r + 54272);
  SB();
  MF(a2[0][0], "v", w1f[33], "v", A2[2][0]);
  MF(a2[0][1], "v", w1f[34], "v", A2[2][0]);
  MF(a2[0][2], "a", w1f[35], "v", A2[2][0]);
  SB();
  LD4A_SC1I(w2f[28], l16, sws, w2b + 0 * W2_NT + 5120, 2048);
  SB();
  MF(a2[1][0], "v", w1f[33], "v", A2[2][1]);
  MF(a2[1][1], "v", w1f[34], "v", A2[2][1]);
  MF(a2[1][2], "a", w1f[35], "v", A2[2][1]);
  SB();
  LD4A_SC1I(w2f[32], l16, sws, w2b + 0 * W2_NT + 5120, 3072);
  SB();
  A2[2][0] = lds_rd4(a2r + 57344);
  A2[2][1] = lds_rd4(a2r + 58368);
  SB();
  ROWSTAT4();
  MF(a2[0][0], "a", w1f[36], "v", A2[0][0]);
  MF(a2[0][1], "a", w1f[37], "v", A2[0][0]);
  MF(a2[0][2], "a", w1f[38], "v", A2[0][0]);
  SB();
  SB();
  MF(a2[1][0], "a", w1f[36], "v", A2[0][1]);
  MF(a2[1][1], "a", w1f[37], "v", A2[0][1]);
  MF(a2[1][2], "a", w1f[38], "v", A2[0][1]);
  SB();
  SB();
  A2[0][0] = lds_rd4(a2r + 61440);
  A2[0][1] = lds_rd4(a2r + 62464);
  SB();
  MF(a2[0][0], "a", w1f[39], "v", A2[1][0]);
  MF(a2[0][1], "a", w1f[40], "v", A2[1][0]);
  MF(a2[0][2], "a", w1f[41], "v", A2[1][0]);
  SB();
  SB();
  MF(a2[1][0], "a", w1f[39], "v", A2[1][1]);
  MF(a2[1][1], "a", w1f[40], "v", A2[1][1]);
  MF(a2[1][2], "a", w1f[41], "v", A2[1][1]);
  SB();
  SB();
  A2[1][0] = lds_rd4(a2r + 2048);
  A2[1][1] = lds_rd4(a2r + 3072);
  SB();
  MF(a2[0][0], "a", w1f[42], "v", A2[2][0]);
  kb[0][0] = c2[0] - rm[0].y * c1[0]; PINV(kb[0][0]);
  SB();
  MF(a2[0][1], "a", w1f[43], "v", A2[2][0]);
  kb[1][0] = c2[0] - rm[1].y * c1[0]; PINV(kb[1][0]);
  SB();
  MF(a2[0][2], "a", w1f[44], "v", A2[2][0]);
  kb[0][1] = c2[1] - rm[0].y * c1[1]; PINV(kb[0][1]);
  SB();
  SB();
  SB();
  MF(a2[1][0], "a", w1f[42], "v", A2[2][1]);
  kb[1][1] = c2[1] - rm[1].y * c1[1]; PINV(kb[1][1]);
  SB();
  MF(a2[1][1], "a", w1f[43], "v", A2[2][1]);
  kb[0][2] = c2[2] - rm[0].y * c1[2]; PINV(kb[0][2]);
  SB();
  MF(a2[1][2], "a", w1f[44], "v", A2[2][1]);
  kb[1][2] = c2[2] - rm[1].y * c1[2]; PINV(kb[1][2]);
  SB();
  SB();
  SB();
  A2[2][0] = lds_rd4(a2r + 6144);
  A2[2][1] = lds_rd4(a2r + 7168);
  SB();
  MF(a2[0][0], "a", w1f[45], "v", A2[0][0]);
  MF(a2[0][1], "a", w1f[46], "v", A2[0][0]);
  MF(a2[0][2], "a", w1f[47], "v", A2[0][0]);
  SB();
  SB();
  MF(a2[1][0], "a", w1f[45], "v", A2[0][1]);
  MF(a2[1][1], "a", w1f[46], "v", A2[0][1]);
  MF(a2[1][2], "a", w1f[47], "v", A2[0][1]);
  SB();
  SB();
  A2[0][0] = lds_rd4(a2r + 10240);
  A2[0][1] = lds_rd4(a2r + 11264);
  SB();
  MF0(a2[2][0], "a", w1f[0], "v", A2[1][0]);
  asm volatile("s_nop 4" : "+a"(a2[0][0]), "+a"(a2[1][0]));
  SB();
  MF0(a2[2][1], "a", w1f[1], "v", A2[1][0]);
  UT1(0, 0);
  SB();
  MF0(a2[2][2], "a", w1f[2], "v", A2[1][0]);
  UT2(ux);
  SB();
  SB();
  SB();
  MF0(a2[3][0], "a", w1f[0], "v", A2[1][1]);
  UT1(1, 0);
  SB();
  MF0(a2[3][1], "a", w1f[1], "v", A2[1][1]);
  UT2(uy);
  SB();
  MF0(a2[3][2], "a", w1f[2], "v", A2[1][1]);
  pq = swap_pack(ux, uy); PINV(pq);
  SB();
  SB();
  ST4_SC1(pq, vu, sws, us0);
  SB();
  A2[1][0] = lds_rd4(a2r + 14336);
  A2[1][1] = lds_rd4(a2r + 15360);
  SB();
  MF(a2[2][0], "a", w1f[3], "v", A2[2][0]);
  asm volatile("" : "+a"(a2[0][1]), "+a"(a2[1][1]));
  SB();
  MF(a2[2][1], "a", w1f[4], "v", A2[2][0]);
  UT1(0, 1);
  SB();
  MF(a2[2][2], "a", w1f[5], "v", A2[2][0]);
  UT2(ux);
  SB();
  SB();
  LD4A_SC1I(w2f[36], l16, sws, w2b + 0 * W2_NT + 9216, 0);
  SB();
  MF(a2[3][0], "a", w1f[3], "v", A2[2][1]);
  UT1(1, 1);
  SB();
  MF(a2[3][1], "a", w1f[4], "v", A2[2][1]);
  UT2(uy);
  SB();
  MF(a2[3][2], "a", w1f[5], "v", A2[2][1]);
  pq = swap_pack(ux, uy); PINV(pq);
  SB();
  SB();
  ST4_SC1(pq, vu, sws, us1);
  SB();
  A2[2][0] = lds_rd4(a2r + 18432);
  A2[2][1] = lds_rd4(a2r + 19456);
  SB();
  MF(a2[2][0], "a", w1f[6], "v", A2[0][0]);
  asm volatile("" : "+a"(a2[0][2]), "+a"(a2[1][2]));
  SB();
  MF(a2[2][1], "a", w1f[7], "v", A2[0][0]);
  UT1(0, 2);
  SB();
  MF(a2[2][2], "v", w1f[8], "v", A2[0][0]);
  UT2(ux);
  SB();
  SB();
  LD4A_SC1I(w2f[21], l16, sws, w2b + 1 * W2_NT + 5120, 0);
  SB();
  MF(a2[3][0], "a", w1f[6], "v", A2[0][1]);
  UT1(1, 2);
  SB();
  MF(a2[3][1], "a", w1f[7], "v", A2[0][1]);
  UT2(uy);
  SB();
  MF(a2[3][2], "v", w1f[8], "v", A2[0][1]);
  pq = swap_pack(ux, uy); PINV(pq);
  SB();
  SB();
  ST4_SC1(pq, vu, sws, us2);
  SB();
  A2[0][0] = lds_rd4(a2r + 22528);
  A2[0][1] = lds_rd4(a2r + 23552);
  SB();
  MF(a2[2][0], "a", w1f[9], "v", A2[1][0]);
  MF(a2[2][1], "a", w1f[10], "v", A2[1][0]);
  MF(a2[2][2], "v", w1f[11], "v", A2[1][0]);
  SB();
  LD4A_SC1I(w2f[25], l16, sws, w2b + 1 * W2_NT + 5120, 1024);
  SB();
  MF(a2[3][0], "a", w1f[9], "v", A2[1][1]);
  MF(a2[3][1], "a", w1f[10], "v", A2[1][1]);
  MF(a2[3][2], "v", w1f[11], "v", A2[1][1]);
  SB();
  LD4A_SC1I(w2f[29], l16, sws, w2b + 1 * W2_NT + 5120, 2048);
  SB();
  A2[1][0] = lds_rd4(a2r + 26624);
  A2[1][1] = lds_rd4(a2r + 27648);
  SB();
  MF(a2[2][0], "v", w1f[12], "v", A2[2][0]);
  MF(a2[2][1], "v", w1f[13], "v", A2[2][0]);
  MF(a2[2][2], "v", w1f[14], "v", A2[2][0]);
  SB();
  LD4A_SC1I(w2f[33], l16, sws, w2b + 1 * W2_NT + 5120, 3072);
  SB();
  MF(a2[3][0], "v", w1f[12], "v", A2[2][1]);
  MF(a2[3][1], "v", w1f[13], "v", A2[2][1]);
  MF(a2[3][2], "v", w1f[14], "v", A2[2][1]);
  SB();
  SB();
  A2[2][0] = lds_rd4(a2r + 30720);
  A2[2][1] = lds_rd4(a2r + 31744);
  SB();
  MF(a2[2][0], "v", w1f[15], "v", A2[0][0]);
  MF(a2[2][1], "v", w1f[16], "v", A2[0][0]);
  MF(a2[2][2], "v", w1f[17], "v", A2[0][0]);
  SB();
  LD4A_SC1I(w2f[37], l16, sws, w2b + 1 * W2_NT + 9216, 0);
  SB();
  MF(a2[3][0], "v", w1f[15], "v", A2[0][1]);
  MF(a2[3][1], "v", w1f[16], "v", A2[0][1]);
  MF(a2[3][2], "v", w1f[17], "v", A2[0][1]);
  SB();
  LD4A_SC1I(w2f[22], l16, sws, w2b + 2 * W2_NT + 5120, 0);
  SB();
  A2[0][0] = lds_rd4(a2r + 34816);
  A2[0][1] = lds_rd4(a2r + 35840);
  SB();
  MF(a2[2][0], "v", w1f[18], "v", A2[1][0]);
  MF(a2[2][1], "v", w1f[19], "v", A2[1][0]);
  MF(a2[2][2], "v", w1f[20], "v", A2[1][0]);
  SB();
  LD4A_SC1I(w2f[26], l16, sws, w2b + 2 * W2_NT + 5120, 1024);
  SB();
  MF(a2[3][0], "v", w1f[18], "v", A2[1][1]);
  MF(a2[3][1], "v", w1f[19], "v", A2[1][1]);
  MF(a2[3][2], "v", w1f[20], "v", A2[1][1]);
  SB();
  SB();
  A2[1][0] = lds_rd4(a2r + 38912);
  A2[1][1] = lds_rd4(a2r + 39936);
  SB();
  MF(a2[2][0], "v", w1f[21], "v", A2[2][0]);
  MF(a2[2][1], "v", w1f[22], "v", A2[2][0]);
  MF(a2[2][2], "v", w1f[23], "v", A2[2][0]);
  SB();
  LD4A_SC1I(w2f[30], l16, sws, w2b + 2 * W2_NT + 5120, 2048);
  SB();
  MF(a2[3][0], "v", w1f[21], "v", A2[2][1]);
  MF(a2[3][1], "v", w1f[22], "v", A2[2][1]);
  MF(a2[3][2], "v", w1f[23], "v", A2[2][1]);
  SB();
  LD4A_SC1I(w2f[34], l16, sws, w2b + 2 * W2_NT + 5120, 3072);
  SB();
  A2[2][0] = lds_rd4(a2r + 43008);
  A2[2][1] = lds_rd4(a2r + 44032);
  SB();
  MF(a2[2][0], "v", w1f[24], "v", A2[0][0]);
  kb[0][0] = c2[0] - rm[2].y * c1[0]; PINV(kb[0][0]);
  SB();
  MF(a2[2][1], "v", w1f[25], "v", A2[0][0]);
  kb[1][0] = c2[0] - rm[3].y * c1[0]; PINV(kb[1][0]);
  SB();
  MF(a2[2][2], "v", w1f[26], "v", A2[0][0]);
  kb[0][1] = c2[1] - rm[2].y * c1[1]; PINV(kb[0][1]);
  SB();
  SB();
  LD4A_SC1I(w2f[38], l16, sws, w2b + 2 * W2_NT + 9216, 0);
  SB();
  MF(a2[3][0], "v", w1f[24], "v", A2[0][1]);
  kb[1][1] = c2[1] - rm[3].y * c1[1]; PINV(kb[1][1]);
  SB();
  MF(a2[3][1], "v", w1f[25], "v", A2[0][1]);
  kb[0][2] = c2[2] - rm[2].y * c1[2]; PINV(kb[0][2]);
  SB();
  MF(a2[3][2], "v", w1f[26], "v", A2[0][1]);
  kb[1][2] = c2[2] - rm[3].y * c1[2]; PINV(kb[1][2]);
  SB();
  SB();
  SB();
  WAITV(9);
  BARN();
  ST1_SC1(tag, lane0, sws, flo + 3072);
  A2[0][0] = lds_rd4(a2r + 47104);
  A2[0][1] = lds_rd4(a2r + 48128);
  SB();
  MF(a2[2][0], "v", w1f[27], "v", A2[1][0]);
  MF(a2[2][1], "v", w1f[28], "v", A2[1][0]);
  MF(a2[2][2], "v", w1f[29], "v", A2[1][0]);
  SB();
  LD4A_SC1I(w2f[23], l16, sws, w2b + 3 * W2_NT + 5120, 0);
  SB();
  MF(a2[3][0], "v", w1f[27], "v", A2[1][1]);
  MF(a2[3][1], "v", w1f[28], "v", A2[1][1]);
  MF(a2[3][2], "v", w1f[29], "v", A2[1][1]);
  SB();
  LD4A_SC1I(w2f[27], l16, sws, w2b + 3 * W2_NT + 5120, 1024);
  SB();
  A2[1][0] = lds_rd4(a2r + 51200);
  A2[1][1] = lds_rd4(a2r + 52224);
  SB();
  MF(a2[2][0], "v", w1f[30], "v", A2[2][0]);
  MF(a2[2][1], "v", w1f[31], "v", A2[2][0]);
  MF(a2[2][2], "a", w1f[32], "v", A2[2][0]);
  SB();
  LD4A_SC1I(w2f[31], l16, sws, w2b + 3 * W2_NT + 5120, 2048);
  SB();
  MF(a2[3][0], "v", w1f[30], "v", A2[2][1]);
  MF(a2[3][1], "v", w1f[31], "v", A2[2][1]);
  MF(a2[3][2], "a", w1f[32], "v", A2[2][1]);
  SB();
  SB();
  A2[2][0] = lds_rd4(a2r + 55296);
  A2[2][1] = lds_rd4(a2r + 56320);
  SB();
  MF(a2[2][0], "v", w1f[33], "v", A2[0][0]);
  MF(a2[2][1], "v", w1f[34], "v", A2[0][0]);
  MF(a2[2][2], "a", w1f[35], "v", A2[0][0]);
  SB();
  LD4A_SC1I(w2f[35], l16, sws, w2b + 3 * W2_NT + 5120, 3072);
  SB();
  MF(a2[3][0], "v", w1f[33], "v", A2[0][1]);
  MF(a2[3][1], "v", w1f[34], "v", A2[0][1]);
  MF(a2[3][2], "a", w1f[35], "v", A2[0][1]);
  SB();
  LD4A_SC1I(w2f[39], l16, sws, w2b + 3 * W2_NT + 9216, 0);
  SB();
  A2[0][0] = lds_rd4(a2r + 59392);
  A2[0][1] = lds_rd4(a2r + 60416);
  SB();
  MF(a2[2][0], "a", w1f[36], "v", A2[1][0]);
  MF(a2[2][1], "a", w1f[37], "v", A2[1][0]);
  MF(a2[2][2], "a", w1f[38], "v", A2[1][0]);
  SB();
  LD4A_SC1I(w2f[40], l16, sws, w2x + 0 * W2_NT + 10240, 0);
  SB();
  MF(a2[3][0], "a", w1f[36], "v", A2[1][1]);
  MF(a2[3][1], "a", w1f[37], "v", A2[1][1]);
  MF(a2[3][2], "a", w1f[38], "v", A2[1][1]);
  SB();
  SB();
  A2[1][0] = lds_rd4(a2r + 63488);
  A2[1][1] = lds_rd4(a2r + 64512);
  SB();
  MF(a2[2][0], "a", w1f[39], "v", A2[2][0]);
  MF(a2[2][1], "a", w1f[40], "v", A2[2][0]);
  MF(a2[2][2], "a", w1f[41], "v", A2[2][0]);
  SB();
  LD4A_SC1I(w2f[41], l16, sws, w2x + 1 * W2_NT + 10240, 0);
  SB();
  MF(a2[3][0], "a", w1f[39], "v", A2[2][1]);
  MF(a2[3][1], "a", w1f[40], "v", A2[2][1]);
  MF(a2[3][2], "a", w1f[41], "v", A2[2][1]);
  SB();
  LD4A_SC1I(w2f[42], l16, sws, w2x + 2 * W2_NT + 10240, 0);
  SB();
  SB();
  MF(a2[2][0], "a", w1f[42], "v", A2[0][0]);
  MF(a2[2][1], "a", w1f[43], "v", A2[0][0]);
  MF(a2[2][2], "a", w1f[44], "v", A2[0][0]);
  SB();
  LD4A_SC1I(w2f[43], l16, sws, w2x + 3 * W2_NT + 10240, 0);
  SB();
  MF(a2[3][0], "a", w1f[42], "v", A2[0][1]);
  MF(a2[3][1], "a", w1f[43], "v", A2[0][1]);
  MF(a2[3][2], "a", w1f[44], "v", A2[0][1]);
  SB();
  SB();
  SB();
  MF(a2[2][0], "a", w1f[45], "v", A2[1][0]);
  MF(a2[2][1], "a", w1f[46], "v", A2[1][0]);
  MF(a2[2][2], "a", w1f[47], "v", A2[1][0]);
  SB();
  SB();
  MF(a2[3][0], "a", w1f[45], "v", A2[1][1]);
  MF(a2[3][1], "a", w1f[46], "v", A2[1][1]);
  MF(a2[3][2], "a", w1f[47], "v", A2[1][1]);
  SB();
  SB();
  LD1V_SC1(f1, fv8, sws, flg + 3072);
  asm volatile("s_nop 6" : "+a"(a2[2][0]), "+a"(a2[2][1]), "+a"(a2[2][2]), "+a"(a2[3][0]), "+a"(a2[3][1]), "+a"(a2[3][2]));
  { v2u X, Y; U_TILE(X, 2, 0); U_TILE(Y, 3, 0); pq = swap_pack(X, Y); }
  ST4_SC1(pq, vu, sws, us0 + 2048);
  { v2u X, Y; U_TILE(X, 2, 1); U_TILE(Y, 3, 1); pq = swap_pack(X, Y); }
  ST4_SC1(pq, vu, sws, us1 + 2048);
  { v2u X, Y; U_TILE(X, 2, 2); U_TILE(Y, 3, 2); pq = swap_pack(X, Y); }
  ST4_SC1(pq, vu, sws, us2 + 2048);
  WAITV(3);
  PINV(f1);
  SPIN(!__all(f1 == tag), POLL1(f1, fv8, flg + 3072));
  LD4V_SC1I(ua[0], l16, sws, uab + 0, 0);
  LD4V_SC1I(ua[1], l16, sws, uab + 0, 1024);
  LD4V_SC1I(ua[4], l16, sws, uab + 4096, 0);
  LD4V_SC1I(ua[5], l16, sws, uab + 4096, 1024);
  LD4V_SC1I(ua[8], l16, sws, uab + 8192, 0);
  LD4V_SC1I(ua[9], l16, sws, uab + 8192, 1024);
  LD4V_SC1I(ua[12], l16, sws, uab + 12288, 0);
  LD4V_SC1I(ua[13], l16, sws, uab + 12288, 1024);
  LD4V_SC1I(ua[16], l16, sws, uab + 16384, 0);
  LD4V_SC1I(ua[17], l16, sws, uab + 16384, 1024);
  LD4V_SC1I(ua[20], l16, sws, uab + 20480, 0);
  LD4V_SC1I(ua[21], l16, sws, uab + 20480, 1024);
  LD4V_SC1I(ua[24], l16, sws, uab + 24576, 0);
  LD4V_SC1I(ua[25], l16, sws, uab + 24576, 1024);
  LD4V_SC1I(ua[28], l16, sws, uab + 28672, 0);
  LD4V_SC1I(ua[29], l16, sws, uab + 28672, 1024);
  WAITV(16);
  BAR();
  ST1_SC1(tag, lane0, sws, flo + 4096);
  PINA(w2f[0]);
  PINA(w2f[1]);
  PINA(w2f[2]);
  PINA(w2f[3]);
  PINA(w2f[4]);
  PINA(w2f[5]);
  PINA(w2f[6]);
  PINA(w2f[7]);
  PINA(w2f[8]);
  PINA(w2f[9]);
  PINA(w2f[10]);
  PINA(w2f[11]);
  PINA(w2f[12]);
  PINA(w2f[13]);
  PINA(w2f[14]);
  PINA(w2f[15]);
  PINA(w2f[16]);
  PINA(w2f[17]);
  PINA(w2f[18]);
  PINA(w2f[19]);
  PINA(w2f[20]);
  PINA(w2f[21]);
  PINA(w2f[22]);
  PINA(w2f[23]);
  PINA(w2f[24]);
  PINA(w2f[25]);
  PINA(w2f[26]);
  PINA(w2f[27]);
  PINA(w2f[28]);
  PINA(w2f[29]);
  PINA(w2f[30]);
  PINA(w2f[31]);
  PINA(w2f[32]);
  PINA(w2f[33]);
  PINA(w2f[34]);
  PINA(w2f[35]);
  PINA(w2f[36]);
  PINA(w2f[37]);
  PINA(w2f[38]);
  PINA(w2f[39]);
  PINA(w2f[40]);
  PINA(w2f[41]);
  PINA(w2f[42]);
  PINA(w2f[43]);
  gr[0] = lds_rd2(mcol + 5120);
  gr[1] = lds_rd2(mcol + 5152);
  gr[2] = lds_rd2(mcol + 5184);
  gr[3] = lds_rd2(mcol + 5216);
  UNPK(gv[0], gr[0]);
  UNPK(gv[1], gr[1]);
  UNPK(gv[2], gr[2]);
  UNPK(gv[3], gr[3]);
  WAITV(15);
  PINV(ua[0]);
  PINV(ua[1]);
  MF0(a3[0][0], "a", w2f[0], "v", ua[0]);
  MF0(a3[0][1], "a", w2f[1], "v", ua[0]);
  MF0(a3[0][2], "a", w2f[2], "v", ua[0]);
  MF0(a3[0][3], "a", w2f[3], "v", ua[0]);
  SB();
  LD4V_SC1I(ua[32], l16, sws, uab + 32768, 0);
  SB();
  MF0(a3[1][0], "a", w2f[0], "v", ua[1]);
  MF0(a3[1][1], "a", w2f[1], "v", ua[1]);
  MF0(a3[1][2], "a", w2f[2], "v", ua[1]);
  MF0(a3[1][3], "a", w2f[3], "v", ua[1]);
  SB();
  LD4V_SC1I(ua[33], l16, sws, uab + 32768, 1024);
  SB();
  WAITV(15);
  PINV(ua[4]);
  PINV(ua[5]);
  MF(a3[0][0], "a", w2f[4], "v", ua[4]);
  MF(a3[0][1], "a", w2f[5], "v", ua[4]);
  MF(a3[0][2], "a", w2f[6], "v", ua[4]);
  MF(a3[0][3], "a", w2f[7], "v", ua[4]);
  SB();
  LD4V_SC1I(ua[36], l16, sws, uab + 36864, 0);
  SB();
  MF(a3[1][0], "a", w2f[4], "v", ua[5]);
  MF(a3[1][1], "a", w2f[5], "v", ua[5]);
  MF(a3[1][2], "a", w2f[6], "v", ua[5]);
  MF(a3[1][3], "a", w2f[7], "v", ua[5]);
  SB();
  LD4V_SC1I(ua[37], l16, sws, uab + 36864, 1024);
  SB();
  WAITV(15);
  PINV(ua[8]);
  PINV(ua[9]);
  MF(a3[0][0], "a", w2f[8], "v", ua[8]);
  MF(a3[0][1], "a", w2f[9], "v", ua[8]);
  MF(a3[0][2], "a", w2f[10], "v", ua[8]);
  MF(a3[0][3], "a", w2f[11], "v", ua[8]);
  SB();
  LD4V_SC1I(ua[40], l16, sws, uax + 40960, 0);
  SB();
  MF(a3[1][0], "a", w2f[8], "v", ua[9]);
  MF(a3[1][1], "a", w2f[9], "v", ua[9]);
  MF(a3[1][2], "a", w2f[10], "v", ua[9]);
  MF(a3[1][3], "a", w2f[11], "v", ua[9]);
  SB();
  LD4V_SC1I(ua[41], l16, sws, uax + 40960, 1024);
  SB();
  WAITV(15);
  PINV(ua[12]);
  PINV(ua[13]);
  MF(a3[0][0], "a", w2f[12], "v", ua[12]);
  MF(a3[0][1], "a", w2f[13], "v", ua[12]);
  MF(a3[0][2], "a", w2f[14], "v", ua[12]);
  MF(a3[0][3], "a", w2f[15], "v", ua[12]);
  SB();
  SB();
  MF(a3[1][0], "a", w2f[12], "v", ua[13]);
  MF(a3[1][1], "a", w2f[13], "v", ua[13]);
  MF(a3[1][2], "a", w2f[14], "v", ua[13]);
  MF(a3[1][3], "a", w2f[15], "v", ua[13]);
  SB();
  SB();
  WAITV(13);
  PINV(ua[16]);
  PINV(ua[17]);
  MF(a3[0][0], "a", w2f[16], "v", ua[16]);
  MF(a3[0][1], "a", w2f[17], "v", ua[16]);
  MF(a3[0][2], "a", w2f[18], "v", ua[16]);
  MF(a3[0][3], "a", w2f[19], "v", ua[16]);
  SB();
  SB();
  MF(a3[1][0], "a", w2f[16], "v", ua[17]);
  MF(a3[1][1], "a", w2f[17], "v", ua[17]);
  MF(a3[1][2], "a", w2f[18], "v", ua[17]);
  MF(a3[1][3], "a", w2f[19], "v", ua[17]);
  SB();
  SB();
  WAITV(11);
  PINV(ua[20]);
  PINV(ua[21]);
  MF(a3[0][0], "a", w2f[20], "v", ua[20]);
  MF(a3[0][1], "a", w2f[21], "v", ua[20]);
  MF(a3[0][2], "a", w2f[22], "v", ua[20]);
  MF(a3[0][3], "a", w2f[23], "v", ua[20]);
  SB();
  LD1V_SC1(f1, fv8, sws, flg + 4096);
  SB();
  MF(a3[1][0], "a", w2f[20], "v", ua[21]);
  MF(a3[1][1], "a", w2f[21], "v", ua[21]);
  MF(a3[1][2], "a", w2f[22], "v", ua[21]);
  MF(a3[1][3], "a", w2f[23], "v", ua[21]);
  SB();
  SB();
  WAITV(10);
  PINV(ua[24]);
  PINV(ua[25]);
  MF(a3[0][0], "a", w2f[24], "v", ua[24]);
  MF(a3[0][1], "a", w2f[25], "v", ua[24]);
  MF(a3[0][2], "a", w2f[26], "v", ua[24]);
  MF(a3[0][3], "a", w2f[27], "v", ua[24]);
  SB();
  SB();
  MF(a3[1][0], "a", w2f[24], "v", ua[25]);
  MF(a3[1][1], "a", w2f[25], "v", ua[25]);
  MF(a3[1][2], "a", w2f[26], "v", ua[25]);
  MF(a3[1][3], "a", w2f[27], "v", ua[25]);
  SB();
  SB();
  WAITV(8);
  PINV(ua[28]);
  PINV(ua[29]);
  MF(a3[0][0], "a", w2f[28], "v", ua[28]);
  MF(a3[0][1], "a", w2f[29], "v", ua[28]);
  MF(a3[0][2], "a", w2f[30], "v", ua[28]);
  MF(a3[0][3], "a", w2f[31], "v", ua[28]);
  SB();
  SB();
  MF(a3[1][0], "a", w2f[28], "v", ua[29]);
  MF(a3[1][1], "a", w2f[29], "v", ua[29]);
  MF(a3[1][2], "a", w2f[30], "v", ua[29]);
  MF(a3[1][3], "a", w2f[31], "v", ua[29]);
  SB();
  SB();
  WAITV(5);
  PINV(ua[32]);
  PINV(ua[33]);
  MF(a3[0][0], "a", w2f[32], "v", ua[32]);
  MF(a3[0][1], "a", w2f[33], "v", ua[32]);
  MF(a3[0][2], "a", w2f[34], "v", ua[32]);
  MF(a3[0][3], "a", w2f[35], "v", ua[32]);
  SB();
  SB();
  MF(a3[1][0], "a", w2f[32], "v", ua[33]);
  MF(a3[1][1], "a", w2f[33], "v", ua[33]);
  MF(a3[1][2], "a", w2f[34], "v", ua[33]);
  MF(a3[1][3], "a", w2f[35], "v", ua[33]);
  SB();
  SB();
  WAITV(3);
  PINV(ua[36]);
  PINV(ua[37]);
  MF(a3[0][0], "a", w2f[36], "v", ua[36]);
  MF(a3[0][1], "a", w2f[37], "v", ua[36]);
  MF(a3[0][2], "a", w2f[38], "v", ua[36]);
  MF(a3[0][3], "a", w2f[39], "v", ua[36]);
  SB();
  SB();
  MF(a3[1][0], "a", w2f[36], "v", ua[37]);
  MF(a3[1][1], "a", w2f[37], "v", ua[37]);
  MF(a3[1][2], "a", w2f[38], "v", ua[37]);
  MF(a3[1][3], "a", w2f[39], "v", ua[37]);
  SB();
  SB();
  WAITV(1);
  PINV(ua[40]);
  PINV(ua[41]);
  WAITV(0);
  PINV(f1);
  SPIN(!__all(f1 == tag), POLL1(f1, fv8, flg + 4096));
  LD4V_SC1I(ua[2], l16, sws, uab + 0, 2048);
  LD4V_SC1I(ua[3], l16, sws, uab + 0, 3072);
  LD4V_SC1I(ua[6], l16, sws, uab + 4096, 2048);
  LD4V_SC1I(ua[7], l16, sws, uab + 4096, 3072);
  LD4V_SC1I(ua[10], l16, sws, uab + 8192, 2048);
  LD4V_SC1I(ua[11], l16, sws, uab + 8192, 3072);
  LD4V_SC1I(ua[14], l16, sws, uab + 12288, 2048);
  LD4V_SC1I(ua[15], l16, sws, uab + 12288, 3072);
  LD4V_SC1I(ua[18], l16, sws, uab + 16384, 2048);
  LD4V_SC1I(ua[19], l16, sws, uab + 16384, 3072);
  MF(a3[0][0], "a", w2f[40], "v", ua[40]);
  MF(a3[0][1], "a", w2f[41], "v", ua[40]);
  MF(a3[0][2], "a", w2f[42], "v", ua[40]);
  MF(a3[0][3], "a", w2f[43], "v", ua[40]);
  SB();
  LD4V_SC1I(ua[22], l16, sws, uab + 20480, 2048);
  SB();
  MF(a3[1][0], "a", w2f[40], "v", ua[41]);
  MF(a3[1][1], "a", w2f[41], "v", ua[41]);
  MF(a3[1][2], "a", w2f[42], "v", ua[41]);
  MF(a3[1][3], "a", w2f[43], "v", ua[41]);
  SB();
  LD4V_SC1I(ua[23], l16, sws, uab + 20480, 3072);
  SB();
  WAITV(10);
  PINV(ua[2]);
  PINV(ua[3]);
  MF0(a3[2][0], "a", w2f[0], "v", ua[2]);
  MF0(a3[2][1], "a", w2f[1], "v", ua[2]);
  MF0(a3[2][2], "a", w2f[2], "v", ua[2]);
  MF0(a3[2][3], "a", w2f[3], "v", ua[2]);
  SB();
  LD4V_SC1I(ua[26], l16, sws, uab + 24576, 2048);
  SB();
  MF0(a3[3][0], "a", w2f[0], "v", ua[3]);
  MF0(a3[3][1], "a", w2f[1], "v", ua[3]);
  MF0(a3[3][2], "a", w2f[2], "v", ua[3]);
  MF0(a3[3][3], "a", w2f[3], "v", ua[3]);
  SB();
  LD4V_SC1I(ua[27], l16, sws, uab + 24576, 3072);
  SB();
  WAITV(10);
  PINV(ua[6]);
  PINV(ua[7]);
  MF(a3[2][0], "a", w2f[4], "v", ua[6]);
  MF(a3[2][1], "a", w2f[5], "v", ua[6]);
  MF(a3[2][2], "a", w2f[6], "v", ua[6]);
  MF(a3[2][3], "a", w2f[7], "v", ua[6]);
  SB();
  LD4V_SC1I(ua[30], l16, sws, uab + 28672, 2048);
  asm volatile("" : "+a"(a3[0][0]));
  lds_wr4f(red3 + 0, a3[0][0]);
  asm volatile("" : "+a"(a3[0][1]));
  lds_wr4f(red3 + 1024, a3[0][1]);
  SB();
  MF(a3[3][0], "a", w2f[4], "v", ua[7]);
  MF(a3[3][1], "a", w2f[5], "v", ua[7]);
  MF(a3[3][2], "a", w2f[6], "v", ua[7]);
  MF(a3[3][3], "a", w2f[7], "v", ua[7]);
  SB();
  LD4V_SC1I(ua[31], l16, sws, uab + 28672, 3072);
  asm volatile("" : "+a"(a3[0][2]));
  lds_wr4f(red3 + 2048, a3[0][2]);
  asm volatile("" : "+a"(a3[0][3]));
  lds_wr4f(red3 + 3072, a3[0][3]);
  SB();
  WAITV(10);
  PINV(ua[10]);
  PINV(ua[11]);
  MF(a3[2][0], "a", w2f[8], "v", ua[10]);
  MF(a3[2][1], "a", w2f[9], "v", ua[10]);
  MF(a3[2][2], "a", w2f[10], "v", ua[10]);
  MF(a3[2][3], "a", w2f[11], "v", ua[10]);
  SB();
  LD4V_SC1I(ua[34], l16, sws, uab + 32768, 2048);
  asm volatile("" : "+a"(a3[1][0]));
  lds_wr4f(red3 + 4096, a3[1][0]);
  asm volatile("" : "+a"(a3[1][1]));
  lds_wr4f(red3 + 5120, a3[1][1]);
  SB();
  MF(a3[3][0], "a", w2f[8], "v", ua[11]);
  MF(a3[3][1], "a", w2f[9], "v", ua[11]);
  MF(a3[3][2], "a", w2f[10], "v", ua[11]);
  MF(a3[3][3], "a", w2f[11], "v", ua[11]);
  SB();
  LD4V_SC1I(ua[35], l16, sws, uab + 32768, 3072);
  asm volatile("" : "+a"(a3[1][2]));
  lds_wr4f(red3 + 6144, a3[1][2]);
  asm volatile("" : "+a"(a3[1][3]));
  lds_wr4f(red3 + 7168, a3[1][3]);
  SB();
  WAITV(10);
  PINV(ua[14]);
  PINV(ua[15]);
  MF(a3[2][0], "a", w2f[12], "v", ua[14]);
  MF(a3[2][1], "a", w2f[13], "v", ua[14]);
  MF(a3[2][2], "a", w2f[14], "v", ua[14]);
  MF(a3[2][3], "a", w2f[15], "v", ua[14]);
  SB();
  LD4V_SC1I(ua[38], l16, sws, uab + 36864, 2048);
  SB();
  MF(a3[3][0], "a", w2f[12], "v", ua[15]);
  MF(a3[3][1], "a", w2f[13], "v", ua[15]);
  MF(a3[3][2], "a", w2f[14], "v", ua[15]);
  MF(a3[3][3], "a", w2f[15], "v", ua[15]);
  SB();
  LD4V_SC1I(ua[39], l16, sws, uab + 36864, 3072);
  SB();
  WAITV(10);
  PINV(ua[18]);
  PINV(ua[19]);
  MF(a3[2][0], "a", w2f[16], "v", ua[18]);
  MF(a3[2][1], "a", w2f[17], "v", ua[18]);
  MF(a3[2][2], "a", w2f[18], "v", ua[18]);
  MF(a3[2][3], "a", w2f[19], "v", ua[18]);
  SB();
  LD4V_SC1I(ua[42], l16, sws, uax + 40960, 2048);
  SB();
  MF(a3[3][0], "a", w2f[16], "v", ua[19]);
  MF(a3[3][1], "a", w2f[17], "v", ua[19]);
  MF(a3[3][2], "a", w2f[18], "v", ua[19]);
  MF(a3[3][3], "a", w2f[19], "v", ua[19]);
  SB();
  LD4V_SC1I(ua[43], l16, sws, uax + 40960, 3072);
  SB();
  WAITV(10);
  PINV(ua[22]);
  PINV(ua[23]);
  MF(a3[2][0], "a", w2f[20], "v", ua[22]);
  MF(a3[2][1], "a", w2f[21], "v", ua[22]);
  MF(a3[2][2], "a", w2f[22], "v", ua[22]);
  MF(a3[2][3], "a", w2f[23], "v", ua[22]);
  SB();
  SB();
  MF(a3[3][0], "a", w2f[20], "v", ua[23]);
  MF(a3[3][1], "a", w2f[21], "v", ua[23]);
  MF(a3[3][2], "a", w2f[22], "v", ua[23]);
  MF(a3[3][3], "a", w2f[23], "v", ua[23]);
  SB();
  SB();
  WAITV(8);
  PINV(ua[26]);
  PINV(ua[27]);
  MF(a3[2][0], "a", w2f[24], "v", ua[26]);
  MF(a3[2][1], "a", w2f[25], "v", ua[26]);
  MF(a3[2][2], "a", w2f[26], "v", ua[26]);
  MF(a3[2][3], "a", w2f[27], "v", ua[26]);
  SB();
  SB();
  MF(a3[3][0], "a", w2f[24], "v", ua[27]);
  MF(a3[3][1], "a", w2f[25], "v", ua[27]);
  MF(a3[3][2], "a", w2f[26], "v", ua[27]);
  MF(a3[3][3], "a", w2f[27], "v", ua[27]);
  SB();
  SB();
  WAITV(6);
  PINV(ua[30]);
  PINV(ua[31]);
  MF(a3[2][0], "a", w2f[28], "v", ua[30]);
  MF(a3[2][1], "a", w2f[29], "v", ua[30]);
  MF(a3[2][2], "a", w2f[30], "v", ua[30]);
  MF(a3[2][3], "a", w2f[31], "v", ua[30]);
  SB();
  SB();
  MF(a3[3][0], "a", w2f[28], "v", ua[31]);
  MF(a3[3][1], "a", w2f[29], "v", ua[31]);
  MF(a3[3][2], "a", w2f[30], "v", ua[31]);
  MF(a3[3][3], "a", w2f[31], "v", ua[31]);
  SB();
  SB();
  WAITV(4);
  PINV(ua[34]);
  PINV(ua[35]);
  MF(a3[2][0], "a", w2f[32], "v", ua[34]);
  MF(a3[2][1], "a", w2f[33], "v", ua[34]);
  MF(a3[2][2], "a", w2f[34], "v", ua[34]);
  MF(a3[2][3], "a", w2f[35], "v", ua[34]);
  SB();
  SB();
  MF(a3[3][0], "a", w2f[32], "v", ua[35]);
  MF(a3[3][1], "a", w2f[33], "v", ua[35]);
  MF(a3[3][2], "a", w2f[34], "v", ua[35]);
  MF(a3[3][3], "a", w2f[35], "v", ua[35]);
  SB();
  SB();
  WAITV(2);
  PINV(ua[38]);
  PINV(ua[39]);
  MF(a3[2][0], "a", w2f[36], "v", ua[38]);
  MF(a3[2][1], "a", w2f[37], "v", ua[38]);
  MF(a3[2][2], "a", w2f[38], "v", ua[38]);
  MF(a3[2][3], "a", w2f[39], "v", ua[38]);
  SB();
  SB();
  MF(a3[3][0], "a", w2f[36], "v", ua[39]);
  MF(a3[3][1], "a", w2f[37], "v", ua[39]);
  MF(a3[3][2], "a", w2f[38], "v", ua[39]);
  MF(a3[3][3], "a", w2f[39], "v", ua[39]);
  SB();
  SB();
  WAITV(0);
  PINV(ua[42]);
  PINV(ua[43]);
  MF(a3[2][0], "a", w2f[40], "v", ua[42]);
  MF(a3[2][1], "a", w2f[41], "v", ua[42]);
  MF(a3[2][2], "a", w2f[42], "v", ua[42]);
  MF(a3[2][3], "a", w2f[43], "v", ua[42]);
  SB();
  SB();
  MF(a3[3][0], "a", w2f[40], "v", ua[43]);
  MF(a3[3][1], "a", w2f[41], "v", ua[43]);
  MF(a3[3][2], "a", w2f[42], "v", ua[43]);
  MF(a3[3][3], "a", w2f[43], "v", ua[43]);
  SB();
  asm volatile("s_nop 3" : "+a"(a3[2][0]), "+a"(a3[2][1]), "+a"(a3[2][2]), "+a"(a3[2][3]));
  lds_wr4f(red3 + 8192, a3[2][0]);
  lds_wr4f(red3 + 9216, a3[2][1]);
  lds_wr4f(red3 + 10240, a3[2][2]);
  lds_wr4f(red3 + 11264, a3[2][3]);
  SB();
  asm volatile("s_nop 3" : "+a"(a3[3][0]), "+a"(a3[3][1]), "+a"(a3[3][2]), "+a"(a3[3][3]));
  lds_wr4f(red3 + 12288, a3[3][0]);
  lds_wr4f(red3 + 13312, a3[3][1]);
  lds_wr4f(red3 + 14336, a3[3][2]);
  lds_wr4f(red3 + 15360, a3[3][3]);
  BAR();
  v4f p3[4][4];
  p3[0][0] = lds_rd4f(rd3 + 0);
  p3[0][1] = lds_rd4f(rd3 + 1024);
  p3[0][2] = lds_rd4f(rd3 + 2048);
  p3[0][3] = lds_rd4f(rd3 + 3072);
  p3[1][0] = lds_rd4f(rd3 + 16384);
  p3[1][1] = lds_rd4f(rd3 + 17408);
  p3[1][2] = lds_rd4f(rd3 + 18432);
  p3[1][3] = lds_rd4f(rd3 + 19456);
  p3[2][0] = lds_rd4f(rd3 + 32768);
  p3[2][1] = lds_rd4f(rd3 + 33792);
  p3[2][2] = lds_rd4f(rd3 + 34816);
  p3[2][3] = lds_rd4f(rd3 + 35840);
  p3[3][0] = lds_rd4f(rd3 + 49152);
  p3[3][1] = lds_rd4f(rd3 + 50176);
  p3[3][2] = lds_rd4f(rd3 + 51200);
  p3[3][3] = lds_rd4f(rd3 + 52224);
  SB();
  G3_EPI();
  pq = swap_pack(op[0], op[1]);
  ST4(pq, vout, sout, (tb * 64 + 16 * w) * 1024 + j * 128 + 0);
  pq = swap_pack(op[2], op[3]);
  ST4(pq, vout, sout, (tb * 64 + 16 * w) * 1024 + j * 128 + 64);

}

extern "C" void kernel_launch(void* const* d_in, const int* in_sizes, int n_in, void* d_out, int out_size, void* d_ws,
                              size_t ws_size, hipStream_t stream) {
  (void)in_sizes; (void)n_in; (void)out_size; (void)ws_size;
  (void)hipFuncSetAttribute((const void*)fused_kernel, hipFuncAttributeMaxDynamicSharedMemorySize, LDS_BYTES);
  fused_kernel<<<256, 256, LDS_BYTES, stream>>>(d_in[0], d_in[1], d_in[2], d_in[3], d_in[4], d_in[6], d_in[7], d_in[8],
                                                d_in[9], d_out, d_ws);
}
```
